```python
import torch

def _softmax(x, axis=-1):
    return torch.softmax(x, dim=axis)
def _gelu(x):
    return torch.nn.functional.gelu(x, approximate="tanh")
def _silu(x):
    return torch.nn.functional.silu(x)
def _sigmoid(x):
    return torch.sigmoid(x)
def _layernorm(x, axis=-1, eps=1e-5):
    m = x.mean(dim=axis, keepdim=True)
    v = x.var(dim=axis, keepdim=True, unbiased=False)
    return (x - m) / torch.sqrt(v + eps)
def _scatter_add(tgt, idx, src, axis=0):
    return tgt.clone().index_add_(axis, idx, src)
def _segment_sum(src, seg, n):
    out = torch.zeros((n,) + src.shape[1:], dtype=src.dtype)
    return out.index_add_(0, seg, src)
def _pad(x, pw):
    flat = tuple(v for pair in reversed(pw) for v in pair)
    return torch.nn.functional.pad(x, flat)

kernel_name = 'gated_mix__sliding_block_cached__geglu_ffn__decode'

def setup_inputs():

    _w = (lambda a: (lambda t: t.to(torch.bfloat16) if t.is_floating_point() else t)(torch.as_tensor(a)))
    def _A_setup():
        B, Sq, D = 2, 1, 1024
        Hq, Hkv, Sk, Dh = 32, 4, 32768, 128
        assert Hq % Hkv == 0, (Hq, Hkv)
        return {
            "x": _w((lambda *s: torch.randn(*s, dtype=torch.bfloat16) * 0.05)(B, Sq, D)),
            "k_cache": _w((lambda *s: torch.randn(*s, dtype=torch.bfloat16) * 6.4)(B, Hkv, Sk, Dh)),
            "v_cache": _w((lambda *s: torch.randn(*s, dtype=torch.bfloat16) * 0.4)(B, Hkv, Sk, Dh)),
            "wq": _w((lambda *s: torch.randn(*s, dtype=torch.bfloat16) * 6.4)(D, Hq * Dh) * (D ** -0.5)),
            "wo": _w((lambda *s: torch.randn(*s, dtype=torch.bfloat16) * 0.4)(Hq * Dh, D) * ((Hq * Dh) ** -0.5)),
        }
    _A_in = _A_setup()

    def _B_setup():
        B, S, D, F = 2, 1, 1024, 3584
        return {
            "x": _w((lambda *s: torch.randn(*s, dtype=torch.bfloat16) * 0.05)(B, S, D)),
            "w_gate": _w((lambda *s: torch.randn(*s, dtype=torch.bfloat16) * 0.05)(D, F)),
            "w_up": _w((lambda *s: torch.randn(*s, dtype=torch.bfloat16) * 0.05)(D, F)),
            "w_down": _w((lambda *s: torch.randn(*s, dtype=torch.bfloat16) * 0.05)(F, D)),
        }
    _B_in = _B_setup()

    return {
        "A_x": _A_in["x"],
        "A_k_cache": _A_in["k_cache"],
        "A_v_cache": _A_in["v_cache"],
        "A_wq": _A_in["wq"],
        "A_wo": _A_in["wo"],
        "B_x": _B_in["x"],
        "B_w_gate": _B_in["w_gate"],
        "B_w_up": _B_in["w_up"],
        "B_w_down": _B_in["w_down"],
        "gate_w": _w((lambda *s: torch.randn(*s, dtype=torch.bfloat16) * 0.05)(1024) * 16.0),
    }

def reference(A_x, A_k_cache, A_v_cache, A_wq, A_wo, B_x, B_w_gate, B_w_up, B_w_down, gate_w):
    def _A_ref(x, k_cache, v_cache, wq, wo):
        B, Sq, D = x.shape
        Hq, Dh = 32, 128
        Hkv, Sk = k_cache.shape[1], k_cache.shape[2]
        W = min(4096, max(256, Sk // 4))
        rep = Hq // Hkv
        q = (lambda x, *d: x.permute(*d))((x @ wq).reshape(B, Sq, Hq, Dh), 0, 2, 1, 3)
        k = (lambda x, n, axis: torch.repeat_interleave(x, n, dim=axis))(k_cache[:, :, Sk - W:, :], rep, axis=1)
        v = (lambda x, n, axis: torch.repeat_interleave(x, n, dim=axis))(v_cache[:, :, Sk - W:, :], rep, axis=1)
        scores = torch.einsum("bhqd,bhkd->bhqk", q, k) * (Dh ** -0.5)
        p = _softmax(scores, axis=-1)
        o = torch.einsum("bhqk,bhkd->bhqd", p, v)
        return (lambda x, *d: x.permute(*d))(o, 0, 2, 1, 3).reshape(B, Sq, Hq * Dh) @ wo

    def _B_ref(x, w_gate, w_up, w_down):
        return (_gelu(x @ w_gate) * (x @ w_up)) @ w_down

    x = A_x
    g = _sigmoid((x * gate_w).sum(-1, keepdims=True))
    a = _A_ref(x, A_k_cache, A_v_cache, A_wq, A_wo)
    b = _B_ref(x, B_w_gate, B_w_up, B_w_down)
    return g * a + (1 - g) * b
```

```cpp
#include <hip/hip_runtime.h>
#include <hip/hip_bf16.h>

typedef __attribute__((ext_vector_type(4))) unsigned u4;
typedef __attribute__((ext_vector_type(2))) unsigned u2;
typedef __attribute__((ext_vector_type(4))) float f4;
typedef __attribute__((ext_vector_type(2))) float f2;
typedef __attribute__((ext_vector_type(8))) __bf16 bf8;
typedef __attribute__((ext_vector_type(2))) __bf16 bf2;
typedef __amdgpu_buffer_rsrc_t rsrc_t;

#define AUX_NT 2
#define AUX_SC1 16
#define SB() __builtin_amdgcn_sched_barrier(0)
#define BAR() asm volatile("s_waitcnt lgkmcnt(0)\n\ts_barrier" ::: "memory")
#define LGKM0() asm volatile("s_waitcnt lgkmcnt(0)" ::: "memory")

constexpr int D = 1024, HQ = 32, HKV = 4, SK = 32768, DH = 128, WIN = 4096, F = 3584;
constexpr int QP_OFF = 4096;
constexpr int OP_OFF = QP_OFF + 2 * 4 * 8 * 512 * 16;
constexpr int LS_OFF = OP_OFF + 64 * 2 * 32 * 16 * 16;
constexpr int HP_OFF = LS_OFF + 32 * 2 * 32 * 16;
constexpr int RP_OFF = HP_OFF + 112 * 8 * 2 * 32 * 16;
constexpr int WS_BYTES = RP_OFF + 256 * 128 * 4 * 16;
constexpr int SPIN_CAP = 1 << 22;

__device__ __forceinline__ rsrc_t mk(const void* p, unsigned bytes) {
  return __builtin_amdgcn_make_buffer_rsrc((void*)p, 0, (int)bytes, 0x00020000);
}
__device__ __forceinline__ u4 ld_nt(rsrc_t r, int off) { return __builtin_amdgcn_raw_buffer_load_b128(r, off, 0, AUX_NT); }
__device__ __forceinline__ u4 ld_sc(rsrc_t r, int off) { return __builtin_amdgcn_raw_buffer_load_b128(r, off, 0, AUX_SC1); }
__device__ __forceinline__ void st_sc(rsrc_t r, int off, u4 v) { __builtin_amdgcn_raw_buffer_store_b128(v, r, off, 0, AUX_SC1); }
__device__ __forceinline__ float asf(unsigned u) { return __builtin_bit_cast(float, u); }
__device__ __forceinline__ unsigned asu(float f) { return __builtin_bit_cast(unsigned, f); }
__device__ __forceinline__ unsigned pk(float a, float b) {
  f2 v = {a, b};
  return __builtin_bit_cast(unsigned, __builtin_convertvector(v, bf2));
}
__device__ __forceinline__ float blo(unsigned u) { return asf(u << 16); }
__device__ __forceinline__ float bhi(unsigned u) { return asf(u & 0xffff0000u); }
__device__ __forceinline__ f4 mfma(u4 a, u4 b, f4 c) {
  return __builtin_amdgcn_mfma_f32_16x16x32_bf16(__builtin_bit_cast(bf8, a), __builtin_bit_cast(bf8, b), c, 0, 0, 0);
}
__device__ __forceinline__ void tile_mma(u4 a, const u4 (&w)[8], f4 (&acc)[8]) {
#pragma unroll
  for (int j = 0; j < 8; j++) {
    const unsigned sel = (j & 1) ? 0x07060302u : 0x05040100u;
    u4 b;
    b.x = __builtin_amdgcn_perm(w[1][j >> 1], w[0][j >> 1], sel);
    b.y = __builtin_amdgcn_perm(w[3][j >> 1], w[2][j >> 1], sel);
    b.z = __builtin_amdgcn_perm(w[5][j >> 1], w[4][j >> 1], sel);
    b.w = __builtin_amdgcn_perm(w[7][j >> 1], w[6][j >> 1], sel);
    acc[j] = mfma(a, b, acc[j]);
  }
}
template <int N>
__device__ __forceinline__ bool tags_ok(const u4 (&v)[N], unsigned tag) {
  unsigned bad = 0;
#pragma unroll
  for (int i = 0; i < N; i++) bad |= (v[i].y ^ tag) | (v[i].w ^ tag);
  return __builtin_amdgcn_ballot_w64(bad != 0) == 0;
}
template <int N, int STRIDE>
__device__ __forceinline__ void poll_retry(u4 (&v)[N], rsrc_t ws, int off, unsigned tag) {
  if (tags_ok(v, tag)) return;
  for (int spin = 0; spin < SPIN_CAP; spin++) {
    asm volatile("" ::: "memory");
#pragma unroll
    for (int i = 0; i < N; i++) v[i] = ld_sc(ws, off + i * STRIDE);
    if (tags_ok(v, tag)) return;
    __builtin_amdgcn_s_sleep(1);
  }
}
#define STAMP(k)

struct Lds {
  u4 kt[4][32 * 16];
  float red[4][8 * 128];
  float red2[4][2 * 128 * 2];
  float ml[4][16];
  float gs[4][2];
  unsigned qs[8 * 68];
  float os[4][2][64];
  unsigned short hs[4][64];
};

__global__ __launch_bounds__(256, 1) void fused_kernel(
    const unsigned short* __restrict__ x, const unsigned short* __restrict__ kc, const unsigned short* __restrict__ vc,
    const unsigned short* __restrict__ wq, const unsigned short* __restrict__ wo, const unsigned short* __restrict__ wg,
    const unsigned short* __restrict__ wu, const unsigned short* __restrict__ wd, const unsigned short* __restrict__ gw,
    unsigned short* __restrict__ out, unsigned* wsp) {
  __shared__ Lds L;
  STAMP(0);
  const int t = threadIdx.x, w = __builtin_amdgcn_readfirstlane(t >> 6), l = t & 63, c = l & 15, g = l >> 4;
  const int xc = blockIdx.x & 7, jb = blockIdx.x >> 3;
  const bool ffn = jb < 28;
  const rsrc_t ws = mk(wsp, WS_BYTES);
  const int ab = xc >> 2, akv = xc & 3;
  const size_t kvbase = ((size_t)(ab * HKV + akv) * SK + (SK - WIN) + jb * 128) * DH;
  const rsrc_t rK = mk(kc + kvbase, 128 * DH * 2), rV = mk(vc + kvbase, 128 * DH * 2);
  const rsrc_t rX = mk(x, 2 * D * 2), rGW = mk(gw, D * 2);
  const rsrc_t rQ = mk(wq, D * HQ * DH * 2), rO = mk(wo, HQ * DH * D * 2);
  const rsrc_t rG = mk(wg, ffn ? D * F * 2 : 0), rU = mk(wu, ffn ? D * F * 2 : 0), rD = mk(wd, ffn ? F * D * 2 : 0);

  const unsigned tag = __builtin_amdgcn_raw_buffer_load_b32(ws, 0, 0, AUX_SC1) + 1u;
  const int krow = xc * 128 + w * 32 + g * 8;
  const u4 xa = __builtin_amdgcn_raw_buffer_load_b128(rX, c < 2 ? (c * D + krow) * 2 : 0x7ffffff0, 0, 0);
  u4 wA[8], kk[8], vv[8];
#pragma unroll
  for (int i = 0; i < 8; i++) wA[i] = ld_nt(rQ, (krow + i) * (HQ * DH * 2) + jb * 256 + c * 16);
  const u2 gx0 = __builtin_amdgcn_raw_buffer_load_b64(rX, t * 8, 0, 0);
  const u2 gx1 = __builtin_amdgcn_raw_buffer_load_b64(rX, D * 2 + t * 8, 0, 0);
  const u2 gww = __builtin_amdgcn_raw_buffer_load_b64(rGW, t * 8, 0, 0);
#pragma unroll
  for (int i = 0; i < 8; i++) kk[i] = ld_nt(rK, (w * 32 + i * 4 + g) * 256 + c * 16);
#pragma unroll
  for (int i = 0; i < 8; i++) vv[i] = ld_nt(rV, (w * 32 + (i >> 2) * 16 + g * 4 + (i & 3)) * 256 + c * 16);
  SB();

  {
    f4 acc[8];
#pragma unroll
    for (int j = 0; j < 8; j++) acc[j] = f4{0.f, 0.f, 0.f, 0.f};
    tile_mma(xa, wA, acc);
    STAMP(1);
    if (g == 0) {
#pragma unroll
      for (int b = 0; b < 2; b++) {
        *(f4*)&L.red2[w][b * 128 + c * 8] = f4{acc[0][b], acc[1][b], acc[2][b], acc[3][b]};
        *(f4*)&L.red2[w][b * 128 + c * 8 + 4] = f4{acc[4][b], acc[5][b], acc[6][b], acc[7][b]};
      }
    }
    float s0 = blo(gx0.x) * blo(gww.x) + bhi(gx0.x) * bhi(gww.x) + blo(gx0.y) * blo(gww.y) + bhi(gx0.y) * bhi(gww.y);
    float s1 = blo(gx1.x) * blo(gww.x) + bhi(gx1.x) * bhi(gww.x) + blo(gx1.y) * blo(gww.y) + bhi(gx1.y) * bhi(gww.y);
    BAR();
    if (w < 2) {
      f2 s = *(f2*)&L.red2[0][t * 2];
      s += *(f2*)&L.red2[1][t * 2];
      s += *(f2*)&L.red2[2][t * 2];
      s += *(f2*)&L.red2[3][t * 2];
      st_sc(ws, QP_OFF + (((w * 4 + (jb >> 3)) * 8 + xc) * 512 + (jb & 7) * 64 + l) * 16, u4{asu(s.x), tag, asu(s.y), tag});
    }
    STAMP(2);
#pragma unroll
    for (int m = 1; m < 64; m <<= 1) { s0 += __shfl_xor(s0, m); s1 += __shfl_xor(s1, m); }
    if (l == 0) { L.gs[w][0] = s0; L.gs[w][1] = s1; }
  }
  SB();
#pragma unroll
  for (int i = 0; i < 8; i++) L.kt[w][(i * 4 + g) * 16 + (c ^ ((i * 4 + g) & 15))] = kk[i];
  STAMP(3);
  u4 qp[16], wB[8];
  const int qoff = QP_OFF + (ab * 4 + akv) * 8 * 512 * 16 + t * 16;
#pragma unroll
  for (int i = 0; i < 16; i++) qp[i] = ld_sc(ws, qoff + i * 4096);
  SB();
#pragma unroll
  for (int i = 0; i < 8; i++) wA[i] = ld_nt(rG, (krow + i) * (F * 2) + jb * 256 + c * 16);
#pragma unroll
  for (int i = 0; i < 8; i++) wB[i] = ld_nt(rU, (krow + i) * (F * 2) + jb * 256 + c * 16);
  SB();
  LGKM0();
#pragma unroll
  for (int i = 0; i < 8; i++) kk[i] = L.kt[w][((i >> 2) * 16 + c) * 16 + (((i & 3) * 4 + g) ^ c)];
  SB();
  poll_retry<16, 4096>(qp, ws, qoff, tag);
  STAMP(4);
  const int r64 = (jb >> 2) * 8 + xc;
  const int toff = (r64 * 64 + (w >> 1) * 32 + g * 8) * (D * 2) + (jb & 3) * 512 + (w & 1) * 256 + c * 16;
  u4 wC[8], wE[8];
#pragma unroll
  for (int i = 0; i < 8; i++) wC[i] = ld_nt(rO, toff + i * (D * 2));
  SB();
  {
    float a0 = 0.f, a1 = 0.f, b0 = 0.f, b1 = 0.f;
#pragma unroll
    for (int i = 0; i < 8; i++) {
      a0 += asf(qp[2 * i].x); a1 += asf(qp[2 * i].z);
      b0 += asf(qp[2 * i + 1].x); b1 += asf(qp[2 * i + 1].z);
    }
    L.qs[w * 68 + l] = pk(a0, a1);
    L.qs[(w + 4) * 68 + l] = pk(b0, b1);
  }
  BAR();
  {
    f4 s0 = {0.f, 0.f, 0.f, 0.f}, s1 = s0;
#pragma unroll
    for (int s = 0; s < 4; s++) {
      const u4 qf = *(const u4*)&L.qs[(c & 7) * 68 + s * 16 + g * 4];
      s0 = mfma(kk[s], qf, s0);
      s1 = mfma(kk[4 + s], qf, s1);
    }
    const float cs = 0.08838834764831845f * 1.4426950408889634f;
    s0 *= cs; s1 *= cs;
    float m = fmaxf(fmaxf(fmaxf(s0[0], s0[1]), fmaxf(s0[2], s0[3])), fmaxf(fmaxf(s1[0], s1[1]), fmaxf(s1[2], s1[3])));
    m = fmaxf(m, __shfl_xor(m, 16));
    m = fmaxf(m, __shfl_xor(m, 32));
    float p[8];
#pragma unroll
    for (int r = 0; r < 4; r++) { p[r] = __builtin_amdgcn_exp2f(s0[r] - m); p[4 + r] = __builtin_amdgcn_exp2f(s1[r] - m); }
    float ls = ((p[0] + p[1]) + (p[2] + p[3])) + ((p[4] + p[5]) + (p[6] + p[7]));
    const u4 pa = {pk(p[0], p[1]), pk(p[2], p[3]), pk(p[4], p[5]), pk(p[6], p[7])};
    f4 acc[8];
#pragma unroll
    for (int j = 0; j < 8; j++) acc[j] = f4{0.f, 0.f, 0.f, 0.f};
    tile_mma(pa, vv, acc);
    ls += __shfl_xor(ls, 16);
    ls += __shfl_xor(ls, 32);
    if (l < 8) { L.ml[w][l] = m; L.ml[w][8 + l] = ls; }
    const bool hi = g >= 2;
#pragma unroll
    for (int r = 0; r < 4; r++)
      *(f4*)&L.red[w][((g & 1) * 4 + r) * 128 + c * 8 + (hi ? 4 : 0)] =
          f4{hi ? acc[4][r] : acc[0][r], hi ? acc[5][r] : acc[1][r], hi ? acc[6][r] : acc[2][r], hi ? acc[7][r] : acc[3][r]};
    BAR();
    const int hh = t >> 5;
    const float m0 = L.ml[0][hh], m1 = L.ml[1][hh], m2 = L.ml[2][hh], m3 = L.ml[3][hh];
    const float M = fmaxf(fmaxf(m0, m1), fmaxf(m2, m3));
    const float f0 = __builtin_amdgcn_exp2f(m0 - M), f1 = __builtin_amdgcn_exp2f(m1 - M), f2_ = __builtin_amdgcn_exp2f(m2 - M),
                f3 = __builtin_amdgcn_exp2f(m3 - M);
    const float Lt = f0 * L.ml[0][8 + hh] + f1 * L.ml[1][8 + hh] + f2_ * L.ml[2][8 + hh] + f3 * L.ml[3][8 + hh];
    f4 o = *(f4*)&L.red[0][t * 4] * f0 + *(f4*)&L.red[1][t * 4] * f1 + *(f4*)&L.red[2][t * 4] * f2_ + *(f4*)&L.red[3][t * 4] * f3;
    o *= __builtin_amdgcn_rcpf(Lt);
    const int h = akv * 8 + hh;
    st_sc(ws, OP_OFF + ((((h * 2 + ((t >> 4) & 1)) * 2 + ab) * 32 + jb) * 16 + c) * 16, u4{pk(o[0], o[1]), tag, pk(o[2], o[3]), tag});
    if ((t & 31) == 0) st_sc(ws, LS_OFF + ((h * 2 + ab) * 32 + jb) * 16, u4{asu(M), tag, asu(Lt), tag});
  }
  STAMP(5);
  SB();
  {
    f4 ag[8], au[8];
#pragma unroll
    for (int j = 0; j < 8; j++) { ag[j] = f4{0.f, 0.f, 0.f, 0.f}; au[j] = ag[j]; }
    tile_mma(xa, wA, ag);
    SB();
#pragma unroll
    for (int i = 0; i < 8; i++) wE[i] = ld_nt(rD, toff + i * (D * 2));
    SB();
    tile_mma(xa, wB, au);
    STAMP(6);
    if (g == 0) {
#pragma unroll
      for (int b = 0; b < 2; b++)
#pragma unroll
        for (int j = 0; j < 8; j += 2)
          *(f4*)&L.red2[w][(b * 128 + c * 8 + j) * 2] = f4{ag[j][b], au[j][b], ag[j + 1][b], au[j + 1][b]};
    }
    BAR();
    if (ffn) {
      f2 s = *(f2*)&L.red2[0][t * 2];
      s += *(f2*)&L.red2[1][t * 2];
      s += *(f2*)&L.red2[2][t * 2];
      s += *(f2*)&L.red2[3][t * 2];
      st_sc(ws, HP_OFF + ((((jb * 4 + ((t >> 5) & 3)) * 8 + xc) * 2 + (t >> 7)) * 32 + (t & 31)) * 16, u4{asu(s.x), tag, asu(s.y), tag});
    }
  }
  STAMP(7);
  SB();
  f4 acc[8];
#pragma unroll
  for (int j = 0; j < 8; j++) acc[j] = f4{0.f, 0.f, 0.f, 0.f};
  {
    u4 op[4], lp[1];
    const int ooff = OP_OFF + r64 * 1024 * 16 + t * 16, loff = LS_OFF + (r64 >> 1) * 64 * 16 + l * 16;
    lp[0] = ld_sc(ws, loff);
#pragma unroll
    for (int i = 0; i < 4; i++) op[i] = ld_sc(ws, ooff + i * 4096);
    poll_retry<1, 0>(lp, ws, loff, tag);
    poll_retry<4, 4096>(op, ws, ooff, tag);
    STAMP(8);
    const float ms = asf(lp[0].x);
    float M = ms;
#pragma unroll
    for (int m = 1; m < 32; m <<= 1) M = fmaxf(M, __shfl_xor(M, m));
    float wt = asf(lp[0].z) * __builtin_amdgcn_exp2f(ms - M), tot = wt;
#pragma unroll
    for (int m = 1; m < 32; m <<= 1) tot += __shfl_xor(tot, m);
    wt *= __builtin_amdgcn_rcpf(tot);
    f4 o[2];
#pragma unroll
    for (int b = 0; b < 2; b++) {
      const float w0 = __shfl(wt, b * 32 + w * 4 + g), w1 = __shfl(wt, b * 32 + 16 + w * 4 + g);
      const u4 a = op[2 * b], e = op[2 * b + 1];
      o[b] = f4{blo(a.x), bhi(a.x), blo(a.z), bhi(a.z)} * w0 + f4{blo(e.x), bhi(e.x), blo(e.z), bhi(e.z)} * w1;
#pragma unroll
      for (int k = 0; k < 4; k++) { o[b][k] += __shfl_xor(o[b][k], 16); o[b][k] += __shfl_xor(o[b][k], 32); }
    }
    if (g < 2) *(f4*)&L.os[w][g][c * 4] = g ? o[1] : o[0];
    BAR();
    const int ob = c & 1, od = (w >> 1) * 32 + g * 8;
    f4 lo = *(f4*)&L.os[0][ob][od] + *(f4*)&L.os[1][ob][od] + *(f4*)&L.os[2][ob][od] + *(f4*)&L.os[3][ob][od];
    f4 hi = *(f4*)&L.os[0][ob][od + 4] + *(f4*)&L.os[1][ob][od + 4] + *(f4*)&L.os[2][ob][od + 4] + *(f4*)&L.os[3][ob][od + 4];
    u4 oa = {pk(lo[0], lo[1]), pk(lo[2], lo[3]), pk(hi[0], hi[1]), pk(hi[2], hi[3])};
    if (c & 2) oa = u4{0u, 0u, 0u, 0u};
    tile_mma(oa, wC, acc);
  }
  STAMP(9);
  SB();
  if (ffn) {
    u4 hp[8];
    const int hoff = HP_OFF + (r64 * 2 + (w >> 1)) * 8 * 64 * 16 + l * 16;
#pragma unroll
    for (int i = 0; i < 8; i++) hp[i] = ld_sc(ws, hoff + i * 1024);
    poll_retry<8, 1024>(hp, ws, hoff, tag);
    STAMP(10);
    float gg = 0.f, uu = 0.f;
#pragma unroll
    for (int i = 0; i < 8; i++) { gg += asf(hp[i].x); uu += asf(hp[i].z); }
    const float z2 = 2.0f * 0.7978845608028654f * (gg + 0.044715f * gg * gg * gg);
    const float hv = gg * __builtin_amdgcn_rcpf(1.0f + __builtin_amdgcn_exp2f(-1.4426950408889634f * z2)) * uu;
    L.hs[w][l] = (unsigned short)pk(hv, 0.f);
    LGKM0();
    u4 ha = *(const u4*)&L.hs[w][(c & 1) * 32 + g * 8];
    if (!(c & 2)) ha = u4{0u, 0u, 0u, 0u};
    tile_mma(ha, wE, acc);
  }
  {
    const float z0 = L.gs[0][0] + L.gs[1][0] + L.gs[2][0] + L.gs[3][0], z1 = L.gs[0][1] + L.gs[1][1] + L.gs[2][1] + L.gs[3][1];
    const float g0 = __builtin_amdgcn_rcpf(1.0f + __builtin_amdgcn_exp2f(-1.4426950408889634f * z0));
    const float g1 = __builtin_amdgcn_rcpf(1.0f + __builtin_amdgcn_exp2f(-1.4426950408889634f * z1));
#pragma unroll
    for (int e = 0; e < 2; e++) {
      const f4 v = g == 0 ? acc[e * 4] : g == 1 ? acc[e * 4 + 1] : g == 2 ? acc[e * 4 + 2] : acc[e * 4 + 3];
      const float y0 = g0 * v[0] + (1.0f - g0) * v[2], y1 = g1 * v[1] + (1.0f - g1) * v[3];
      const int red = (((jb & 3) * 2 + (w & 1)) * 8 + e * 4 + g) * 4 + (c >> 2);
      st_sc(ws, RP_OFF + ((red * 128 + r64 * 2 + (w >> 1)) * 4 + (c & 3)) * 16, u4{asu(y0), tag, asu(y1), tag});
    }
  }
  STAMP(11);
  if (w == 0) {
    u4 rp[8];
    const int roff = RP_OFF + blockIdx.x * 8192 + l * 16;
#pragma unroll
    for (int i = 0; i < 8; i++) rp[i] = ld_sc(ws, roff + i * 1024);
    poll_retry<8, 1024>(rp, ws, roff, tag);
    STAMP(12);
    float y0 = 0.f, y1 = 0.f;
#pragma unroll
    for (int i = 0; i < 8; i++) { y0 += asf(rp[i].x); y1 += asf(rp[i].z); }
#pragma unroll
    for (int m = 4; m < 64; m <<= 1) { y0 += __shfl_xor(y0, m); y1 += __shfl_xor(y1, m); }
    const int bid = blockIdx.x;
    const int col = (bid >> 5) * 128 + ((bid & 3) * 4 + (l & 3)) * 8 + ((bid >> 2) & 7);
    if (l < 8) out[(l >> 2) * D + col] = (unsigned short)pk(l < 4 ? y0 : y1, 0.f);
    if (bid == 0 && l == 0) __builtin_amdgcn_raw_buffer_store_b32(tag, ws, 0, 0, AUX_SC1);
    STAMP(13);
  }
}

extern "C" void kernel_launch(void* const* d_in, const int* in_sizes, int n_in, void* d_out, int out_size, void* d_ws,
                              size_t ws_size, hipStream_t stream) {
  typedef const unsigned short* cp;
  fused_kernel<<<256, 256, 0, stream>>>((cp)d_in[0], (cp)d_in[1], (cp)d_in[2], (cp)d_in[3], (cp)d_in[4], (cp)d_in[6],
                                        (cp)d_in[7], (cp)d_in[8], (cp)d_in[9], (unsigned short*)d_out, (unsigned*)d_ws);
}
```

```cpp
#include <hip/hip_runtime.h>
#include <hip/hip_bf16.h>

typedef __attribute__((ext_vector_type(4))) unsigned u4;
typedef __attribute__((ext_vector_type(2))) unsigned u2;
typedef __attribute__((ext_vector_type(4))) float f4;
typedef __attribute__((ext_vector_type(2))) float f2;
typedef __attribute__((ext_vector_type(8))) __bf16 bf8;
typedef __attribute__((ext_vector_type(2))) __bf16 bf2;
typedef __amdgpu_buffer_rsrc_t rsrc_t;

#define AUX_NT 2
#define AUX_SC1 16
#define SB() __builtin_amdgcn_sched_barrier(0)
#define BAR() asm volatile("s_waitcnt lgkmcnt(0)\n\ts_barrier" ::: "memory")
#define LGKM0() asm volatile("s_waitcnt lgkmcnt(0)" ::: "memory")

constexpr int D = 1024, HQ = 32, HKV = 4, SK = 32768, DH = 128, WIN = 4096, F = 3584;
constexpr int QP_OFF = 4096;
constexpr int QF_OFF = QP_OFF + 32 * 8 * 8 * 16 * 16;
constexpr int OP_OFF = QF_OFF + 256 * 4096;
constexpr int LS_OFF = OP_OFF + 128 * 2 * 32 * 8 * 16;
constexpr int HP_OFF = LS_OFF + 128 * 1024;
constexpr int RP_OFF = HP_OFF + 112 * 8 * 2 * 32 * 16;
constexpr int WS_BYTES = RP_OFF + 256 * 128 * 4 * 16;
constexpr int SPIN_CAP = 1 << 20;
#define WA 5
#define WF 5
#define SLP 6
#define SLP1 20
#define WX 2
#define SLP0 8

__device__ __forceinline__ rsrc_t mk(const void* p, unsigned bytes) {
  return __builtin_amdgcn_make_buffer_rsrc((void*)p, 0, (int)bytes, 0x00020000);
}
__device__ __forceinline__ u4 ld_nt(rsrc_t r, int off) { return __builtin_amdgcn_raw_buffer_load_b128(r, off, 0, AUX_NT); }
__device__ __forceinline__ u4 ld_sc(rsrc_t r, int off) { return __builtin_amdgcn_raw_buffer_load_b128(r, off, 0, AUX_SC1); }
__device__ __forceinline__ void st_sc(rsrc_t r, int off, u4 v) { __builtin_amdgcn_raw_buffer_store_b128(v, r, off, 0, AUX_SC1); }
__device__ __forceinline__ float asf(unsigned u) { return __builtin_bit_cast(float, u); }
__device__ __forceinline__ unsigned asu(float f) { return __builtin_bit_cast(unsigned, f); }
__device__ __forceinline__ unsigned pk(float a, float b) {
  f2 v = {a, b};
  return __builtin_bit_cast(unsigned, __builtin_convertvector(v, bf2));
}
__device__ __forceinline__ float blo(unsigned u) { return asf(u << 16); }
__device__ __forceinline__ float bhi(unsigned u) { return asf(u & 0xffff0000u); }
__device__ __forceinline__ f4 mfma(u4 a, u4 b, f4 c) {
  return __builtin_amdgcn_mfma_f32_16x16x32_bf16(__builtin_bit_cast(bf8, a), __builtin_bit_cast(bf8, b), c, 0, 0, 0);
}
__device__ __forceinline__ void tile_mma(u4 a, const u4 (&w)[8], f4 (&acc)[8]) {
#pragma unroll
  for (int j = 0; j < 8; j++) {
    const unsigned sel = (j & 1) ? 0x07060302u : 0x05040100u;
    u4 b;
    b.x = __builtin_amdgcn_perm(w[1][j >> 1], w[0][j >> 1], sel);
    b.y = __builtin_amdgcn_perm(w[3][j >> 1], w[2][j >> 1], sel);
    b.z = __builtin_amdgcn_perm(w[5][j >> 1], w[4][j >> 1], sel);
    b.w = __builtin_amdgcn_perm(w[7][j >> 1], w[6][j >> 1], sel);
    acc[j] = mfma(a, b, acc[j]);
  }
}
template <int N>
__device__ __forceinline__ bool tags_ok(const u4 (&v)[N], unsigned tag) {
  unsigned bad = 0;
#pragma unroll
  for (int i = 0; i < N; i++) bad |= (v[i].y ^ tag) | (v[i].w ^ tag);
  return __builtin_amdgcn_ballot_w64(bad != 0) == 0;
}
template <int N, int STRIDE>
__device__ __forceinline__ void poll(u4 (&v)[N], rsrc_t ws, int off, unsigned tag) {
#pragma nounroll
  for (int spin = 0; spin < SPIN_CAP; spin++) {
    asm volatile("" ::: "memory");
#pragma unroll
    for (int i = 0; i < N; i++) v[i] = ld_sc(ws, off + i * STRIDE);
    if (tags_ok(v, tag)) return;
    __builtin_amdgcn_s_sleep(SLP);
  }
}
__device__ __forceinline__ void to_frags(u4 (&w)[8]) {
  u4 b[8];
#pragma unroll
  for (int j = 0; j < 8; j++) {
    const unsigned sel = (j & 1) ? 0x07060302u : 0x05040100u;
#pragma unroll
    for (int k = 0; k < 4; k++) b[j][k] = __builtin_amdgcn_perm(w[2 * k + 1][j >> 1], w[2 * k][j >> 1], sel);
  }
#pragma unroll
  for (int j = 0; j < 8; j++) { w[j] = b[j]; asm volatile("" : "+v"(w[j])); }
}
template <int CTRL>
__device__ __forceinline__ float dpp(float v) {
  return asf(__builtin_amdgcn_update_dpp(0u, asu(v), CTRL, 0xf, 0xf, true));
}
__device__ __forceinline__ float add32(float a, float b) {
  const u2 r = __builtin_amdgcn_permlane32_swap(asu(a), asu(b), false, false);
  return asf(r.x) + asf(r.y);
}
__device__ __forceinline__ float add16(float a) {
  const u2 r = __builtin_amdgcn_permlane16_swap(asu(a), asu(a), false, false);
  return asf(r.x) + asf(r.y);
}
__device__ __forceinline__ float max32(float a) {
  const u2 r = __builtin_amdgcn_permlane32_swap(asu(a), asu(a), false, false);
  return fmaxf(asf(r.x), asf(r.y));
}
__device__ __forceinline__ float max16(float a) {
  const u2 r = __builtin_amdgcn_permlane16_swap(asu(a), asu(a), false, false);
  return fmaxf(asf(r.x), asf(r.y));
}
__device__ __forceinline__ float row_sum(float v) {
  v += dpp<0x128>(v); v += dpp<0x124>(v); v += dpp<0x122>(v); v += dpp<0x121>(v);
  return v;
}
__device__ __forceinline__ float row_max(float v) {
  v = fmaxf(v, dpp<0x128>(v)); v = fmaxf(v, dpp<0x124>(v)); v = fmaxf(v, dpp<0x122>(v)); v = fmaxf(v, dpp<0x121>(v));
  return v;
}
__device__ __forceinline__ f4 sel4(bool p, f4 a, f4 b) {
  return f4{p ? a.x : b.x, p ? a.y : b.y, p ? a.z : b.z, p ? a.w : b.w};
}
__device__ __forceinline__ float dot8(u4 a, u4 b) {
  float s = 0.f;
#pragma unroll
  for (int i = 0; i < 4; i++) s += blo(a[i]) * blo(b[i]) + bhi(a[i]) * bhi(b[i]);
  return s;
}
#define STAMP(k)
#define DUMP(base)

struct Lds {
  u4 kt[7][32 * 16];
  float red[4][8 * 128];
  float qr[4][2 * 128];
  float gu[4][2 * 128 * 2];
  float ml[4][16];
  float gs[2];
  unsigned tag, spare;
  float fin[4][16];
  unsigned fl[16];
  unsigned qs[8 * 68];
  unsigned os[2][16];
  unsigned short hs[2][32];
};
__device__ __forceinline__ void sig(unsigned* f, int l) {
  LGKM0();
  if (l == 0) __hip_atomic_fetch_add(f, 1u, __ATOMIC_RELAXED, __HIP_MEMORY_SCOPE_WORKGROUP);
  asm volatile("" ::: "memory");
}
template <bool SLEEP = false>
__device__ __forceinline__ void await(unsigned* f, unsigned n) {
#pragma nounroll
  for (int spin = 0; spin < SPIN_CAP * 16; spin++) {
    if (__builtin_amdgcn_readfirstlane(__hip_atomic_load(f, __ATOMIC_RELAXED, __HIP_MEMORY_SCOPE_WORKGROUP)) >= n) break;
    if (SLEEP) __builtin_amdgcn_s_sleep(1);
  }
  asm volatile("" ::: "memory");
}

__global__ __launch_bounds__(640, 1) void fused_kernel(
    const unsigned short* __restrict__ x, const unsigned short* __restrict__ kc, const unsigned short* __restrict__ vc,
    const unsigned short* __restrict__ wq, const unsigned short* __restrict__ wo, const unsigned short* __restrict__ wg,
    const unsigned short* __restrict__ wu, const unsigned short* __restrict__ wd, const unsigned short* __restrict__ gw,
    unsigned short* __restrict__ out, unsigned* wsp) {
  __shared__ Lds L;
  STAMP(0);
  const int t = threadIdx.x, w = __builtin_amdgcn_readfirstlane(t >> 6), l = t & 63, c = l & 15, g = l >> 4;
  const int xc = blockIdx.x & 7, jb = blockIdx.x >> 3;
  const bool ffn = jb < 28;
  const rsrc_t ws = mk(wsp, WS_BYTES);
  const int ab = xc >> 2, akv = xc & 3;
  const int rr = jb * 4 + (xc >> 1);
  const rsrc_t rX = mk(x, 2 * D * 2);
  const f4 zero4 = {0.f, 0.f, 0.f, 0.f};

#define ISSUE(LO, HI, WAIT, LD)                                   \
  _Pragma("unroll") for (int m = (LO); m < (HI); m++) {           \
    const int i = m & 7;                                          \
    SB();                                                         \
    if (WAIT) asm volatile("s_waitcnt vmcnt(%0)" ::"n"((WAIT) - 1)); \
    SB();                                                         \
    T[m >> 3][i] = LD;                                            \
    SB();                                                         \
  }
  const int w4 = w & 3;
  const int krow = xc * 128 + w4 * 32 + g * 8;
  const int toff = (rr * 32 + g * 8) * (D * 2) + (xc & 1) * 1024 + w4 * 256 + c * 16;
  if (w < 4) {
    const size_t kvbase = ((size_t)(ab * HKV + akv) * SK + (SK - WIN) + jb * 128) * DH;
    const rsrc_t rK = mk(kc + kvbase, 128 * DH * 2), rV = mk(vc + kvbase, 128 * DH * 2);
    const rsrc_t rQ = mk(wq, D * HQ * DH * 2);
    const int qoff = krow * (HQ * DH * 2) + jb * 256 + c * 16;
    const int koff = (w * 32 + (g ^ jb)) * 256 + c * 16, voff = (w * 32 + ((g * 4) ^ jb)) * 256 + c * 16;
    u4 T[3][8];
    f4 acc[8];
#define LDA (m < 8 ? ld_nt(rQ, qoff + i * (HQ * DH * 2)) : m < 16 ? ld_nt(rK, koff ^ (i * 1024)) : ld_nt(rV, voff ^ ((i >> 2) * 4096 + (i & 3) * 256)))
    ISSUE(0, 4, 0, LDA)
    const u4 xa = __builtin_amdgcn_raw_buffer_load_b128(rX, c < 2 ? (c * D + krow) * 2 : 0x7ffffff0, 0, 0);
    const unsigned tag = __builtin_amdgcn_raw_buffer_load_b32(ws, 0, 0, AUX_SC1) + 1u;
    ISSUE(4, 8, 0, LDA)
    asm volatile("s_barrier" ::: "memory");
    ISSUE(8, 8 + WA, WA, LDA)
    {
#pragma unroll
      for (int j = 0; j < 8; j++) acc[j] = zero4;
      tile_mma(xa, T[0], acc);
      if (g == 0) {
#pragma unroll
        for (int b = 0; b < 2; b++) {
          *(f4*)&L.qr[w][b * 128 + c * 8] = f4{acc[0][b], acc[1][b], acc[2][b], acc[3][b]};
          *(f4*)&L.qr[w][b * 128 + c * 8 + 4] = f4{acc[4][b], acc[5][b], acc[6][b], acc[7][b]};
        }
      }
      if (t == 0) L.tag = tag;
      sig(&L.fl[0], l);
    }
    STAMP(1);
    ISSUE(8 + WA, 24, WA, LDA)
#pragma unroll
    for (int i = 0; i < 8; i++) L.kt[w][(i * 4 + g) * 16 + (c ^ ((i * 4 + g) & 15))] = T[1][i];
    LGKM0();
#pragma unroll
    for (int i = 0; i < 8; i++) T[1][i] = L.kt[w][((i >> 2) * 16 + c) * 16 + (((i & 3) * 4 + g) ^ c)];
    SB();
    asm volatile("s_waitcnt vmcnt(%0)" ::"n"(WX));
    sig(&L.fl[8 + w], l);
    to_frags(T[2]);
    STAMP(2);
    await(&L.fl[3], 1);
    STAMP(3);
    {
        f4 s0 = zero4, s1 = zero4;
#pragma unroll
        for (int s = 0; s < 4; s++) {
          const u4 qf = *(const u4*)&L.qs[(c & 7) * 68 + s * 16 + g * 4];
          s0 = mfma(T[1][s], qf, s0);
          s1 = mfma(T[1][4 + s], qf, s1);
        }
        const float cs = 0.08838834764831845f * 1.4426950408889634f;
        s0 *= cs; s1 *= cs;
        float m = fmaxf(fmaxf(fmaxf(s0[0], s0[1]), fmaxf(s0[2], s0[3])), fmaxf(fmaxf(s1[0], s1[1]), fmaxf(s1[2], s1[3])));
        m = max32(max16(m));
        float p[8];
#pragma unroll
        for (int r = 0; r < 4; r++) { p[r] = __builtin_amdgcn_exp2f(s0[r] - m); p[4 + r] = __builtin_amdgcn_exp2f(s1[r] - m); }
        float ls = ((p[0] + p[1]) + (p[2] + p[3])) + ((p[4] + p[5]) + (p[6] + p[7]));
        const u4 pa = {pk(p[0], p[1]), pk(p[2], p[3]), pk(p[4], p[5]), pk(p[6], p[7])};
#pragma unroll
        for (int j = 0; j < 8; j++) acc[j] = mfma(pa, T[2][j], zero4);
        ls = add16(ls);
        ls = add32(ls, ls);
        if (l < 8) { L.ml[w][l] = m; L.ml[w][8 + l] = ls; }
        const bool hi = g >= 2;
#pragma unroll
        for (int r = 0; r < 4; r++)
          *(f4*)&L.red[w][((g & 1) * 4 + r) * 128 + c * 8 + (hi ? 4 : 0)] =
              f4{hi ? acc[4][r] : acc[0][r], hi ? acc[5][r] : acc[1][r], hi ? acc[6][r] : acc[2][r], hi ? acc[7][r] : acc[3][r]};
      sig(&L.fl[2], l);
    }
    STAMP(4);
    await(&L.fl[2], 4);
    {
      const int hh = t >> 5;
      const float m0 = L.ml[0][hh], m1 = L.ml[1][hh], m2 = L.ml[2][hh], m3 = L.ml[3][hh];
      const float M = fmaxf(fmaxf(m0, m1), fmaxf(m2, m3));
      const float f0 = __builtin_amdgcn_exp2f(m0 - M), f1 = __builtin_amdgcn_exp2f(m1 - M), f2_ = __builtin_amdgcn_exp2f(m2 - M),
                  f3 = __builtin_amdgcn_exp2f(m3 - M);
      const float Lt = f0 * L.ml[0][8 + hh] + f1 * L.ml[1][8 + hh] + f2_ * L.ml[2][8 + hh] + f3 * L.ml[3][8 + hh];
      f4 o = *(f4*)&L.red[0][t * 4] * f0 + *(f4*)&L.red[1][t * 4] * f1 + *(f4*)&L.red[2][t * 4] * f2_ + *(f4*)&L.red[3][t * 4] * f3;
      o *= __builtin_amdgcn_rcpf(Lt);
      st_sc(ws, OP_OFF + (((((akv * 8 + hh) * 4 + ((l >> 3) & 3)) * 2 + ab) * 32 + jb) * 8 + (l & 7)) * 16,
            u4{pk(o[0], o[1]), tag, pk(o[2], o[3]), tag});
      if ((l & 7) == 0)
        st_sc(ws, LS_OFF + ((((akv * 8 + hh) * 4 + ((l >> 3) & 3)) * 2 + ab) * 32 + jb) * 16, u4{asu(M), tag, asu(Lt), tag});
    }
    STAMP(5);
    if (!(blockIdx.x & 1)) {
      const int red = blockIdx.x >> 1;
      await<true>(&L.fl[6], 4);
      u4 rp[4];
      poll<4, 1024>(rp, ws, RP_OFF + red * 16384 + w * 4096 + l * 16, tag);
      STAMP(6);
      float y = add16(add32((asf(rp[0].x) + asf(rp[1].x)) + (asf(rp[2].x) + asf(rp[3].x)),
                            (asf(rp[0].z) + asf(rp[1].z)) + (asf(rp[2].z) + asf(rp[3].z))));
      y += dpp<0x128>(y);
      if ((l & 24) == 0) L.fin[w][(l >> 5) * 8 + (l & 7)] = y;
      sig(&L.fl[7], l);
      if (w == 0) {
        await(&L.fl[7], 4);
        const int col = (red >> 4) * 128 + ((red & 1) * 8 + (l & 7)) * 8 + ((red >> 1) & 7);
        if (l < 16) out[(l >> 3) * D + col] = (unsigned short)pk((L.fin[0][l] + L.fin[1][l]) + (L.fin[2][l] + L.fin[3][l]), 0.f);
        if (blockIdx.x == 0 && l == 0) __builtin_amdgcn_raw_buffer_store_b32(tag, ws, 0, 0, AUX_SC1);
      }
      STAMP(7);
    }
    if (w == 0) DUMP(0);
  } else if (w < 8) {
    const rsrc_t rG = mk(wg, ffn ? D * F * 2 : 0), rU = mk(wu, ffn ? D * F * 2 : 0), rD = mk(wd, ffn ? F * D * 2 : 0);
    const rsrc_t rO = mk(wo, HQ * DH * D * 2);
    const int goff = krow * (F * 2) + jb * 256 + c * 16;
    u4 T[4][8];
    f4 ag[8], acc[8];
#define LDF (m < 8 ? ld_nt(rG, goff + i * (F * 2)) : m < 16 ? ld_nt(rU, goff + i * (F * 2)) : m < 24 ? ld_nt(rO, toff + i * (D * 2)) : ld_nt(rD, toff + i * (D * 2)))
    const u4 xa = __builtin_amdgcn_raw_buffer_load_b128(rX, c < 2 ? (c * D + krow) * 2 : 0x7ffffff0, 0, 0);
    asm volatile("s_barrier" ::: "memory");
    await<true>(&L.fl[8 + w4], 1);
    ISSUE(0, WF, 0, LDF)
    ISSUE(WF, 8 + WF, WF, LDF)
#pragma unroll
    for (int j = 0; j < 8; j++) ag[j] = zero4;
    tile_mma(xa, T[0], ag);
    ISSUE(8 + WF, 16 + WF, WF, LDF)
#pragma unroll
    for (int j = 0; j < 8; j++) acc[j] = zero4;
    tile_mma(xa, T[1], acc);
    if (g == 0) {
#pragma unroll
      for (int b = 0; b < 2; b++)
#pragma unroll
        for (int j = 0; j < 8; j += 2)
          *(f4*)&L.gu[w4][(b * 128 + c * 8 + j) * 2] = f4{ag[j][b], acc[j][b], ag[j + 1][b], acc[j + 1][b]};
    }
    sig(&L.fl[1], l);
    ISSUE(16 + WF, 32, WF, LDF)
    to_frags(T[2]);
    to_frags(T[3]);
    STAMP(6);
    await(&L.fl[4], 1);
    const unsigned tag = L.tag;
    const float g0 = L.gs[0], g1 = L.gs[1];
    u4 ha = *(const u4*)&L.hs[c & 1][g * 8];
    if (!(c & 2)) ha = u4{0u, 0u, 0u, 0u};
#pragma unroll
    for (int j = 0; j < 8; j++) acc[j] = mfma(ha, T[3][j], zero4);
    await(&L.fl[5], 1);
    u4 oa = *(const u4*)&L.os[c & 1][g * 4];
    if (c & 2) oa = u4{0u, 0u, 0u, 0u};
#pragma unroll
    for (int j = 0; j < 8; j++) acc[j] = mfma(oa, T[2][j], acc[j]);
#pragma unroll
    for (int e = 0; e < 2; e++) {
      const f4 v = sel4(g & 2, sel4(g & 1, acc[e * 4 + 3], acc[e * 4 + 2]), sel4(g & 1, acc[e * 4 + 1], acc[e * 4]));
      const float y0 = g0 * v[0] + (1.0f - g0) * v[2], y1 = g1 * v[1] + (1.0f - g1) * v[3];
      const int red = (((xc & 1) * 4 + w4) * 8 + e * 4 + g) * 2 + (c >> 3);
      st_sc(ws, RP_OFF + ((red * 128 + rr) * 8 + (c & 7)) * 16, u4{asu(y0), tag, asu(y1), tag});
    }
    sig(&L.fl[6], l);
    STAMP(7);
  } else if (w == 8) {
    if (l < 16) L.fl[l] = 0u;
    BAR();
    await(&L.fl[0], 4);
    const unsigned tag = L.tag;
#pragma unroll
    for (int i = 0; i < 2; i++) {
      const int dblk = i * 4 + g, o = dblk * 32 + c * 2;
      const f2 s = (*(f2*)&L.qr[0][o] + *(f2*)&L.qr[1][o]) + (*(f2*)&L.qr[2][o] + *(f2*)&L.qr[3][o]);
      st_sc(ws, QP_OFF + (((jb * 8 + dblk) * 8 + xc) * 16 + c) * 16, u4{asu(s.x), tag, asu(s.y), tag});
    }
    STAMP(1);
    {
      u4 qp[2];
      __builtin_amdgcn_s_sleep(SLP0);
      poll<2, 1024>(qp, ws, QP_OFF + (jb * 8 + xc) * 2048 + l * 16, tag);
      const float a = add16(add32(asf(qp[0].x) + asf(qp[1].x), asf(qp[0].z) + asf(qp[1].z)));
      const u2 r = __builtin_amdgcn_permlane32_swap(asu(a), asu(a), false, false);
      const int fo = QF_OFF + (g * 8 + ab * 4 + (jb >> 3)) * 4096 + ((jb & 7) * 64 + akv * 16 + c) * 8;
#pragma unroll
      for (int i = 0; i < 8; i++)
        __builtin_amdgcn_raw_buffer_store_b64(u2{pk(asf(r.x), asf(r.y)), tag}, ws, fo + i * (32 * 4096), 0, AUX_SC1);
    }
    STAMP(2);
    {
      u4 qf[4];
      __builtin_amdgcn_s_sleep(SLP0);
      poll<4, 1024>(qf, ws, QF_OFF + blockIdx.x * 4096 + l * 16, tag);
#pragma unroll
      for (int i = 0; i < 4; i++) *(u2*)&L.qs[(i * 2 + (l >> 5)) * 68 + (l & 31) * 2] = u2{qf[i].x, qf[i].z};
      sig(&L.fl[3], l);
    }
    STAMP(3);
    await(&L.fl[2], 4);
    STAMP(4);
    {
      u4 lp[1], op[8];
      __builtin_amdgcn_s_sleep(SLP1);
      poll<1, 0>(lp, ws, LS_OFF + rr * 1024 + l * 16, tag);
      STAMP(5);
      const int ooff = OP_OFF + rr * 8192 + l * 16;
#pragma unroll
      for (int i = 0; i < 8; i++) op[i] = ld_sc(ws, ooff + i * 1024);
      SB();
      const float ms = asf(lp[0].x), M = max16(row_max(ms));
      float wt = asf(lp[0].z) * __builtin_amdgcn_exp2f(ms - M);
      wt *= __builtin_amdgcn_rcpf(add16(row_sum(wt)));
      float wi[8];
#pragma unroll
      for (int i = 0; i < 8; i++) wi[i] = asf(__builtin_amdgcn_ds_bpermute((i * 8 + (l >> 3)) * 4, asu(wt)));
      SB();
      if (!tags_ok(op, tag)) poll<8, 1024>(op, ws, ooff, tag);
      STAMP(6);
      f4 o[2] = {zero4, zero4};
#pragma unroll
      for (int i = 0; i < 8; i++) o[i >> 2] += f4{blo(op[i].x), bhi(op[i].x), blo(op[i].z), bhi(op[i].z)} * wi[i];
      float v[4];
#pragma unroll
      for (int k = 0; k < 4; k++) { v[k] = add16(add32(o[0][k], o[1][k])); v[k] += dpp<0x128>(v[k]); }
      if ((l & 24) == 0) *(u2*)&L.os[l >> 5][(l & 7) * 2] = u2{pk(v[0], v[1]), pk(v[2], v[3])};
      sig(&L.fl[5], l);
    }
    STAMP(7);
    DUMP(8);
  } else {
    const rsrc_t rGW = mk(gw, D * 2);
    u4 gx[6];
#pragma unroll
    for (int i = 0; i < 4; i++) gx[i] = __builtin_amdgcn_raw_buffer_load_b128(rX, i * 1024 + l * 16, 0, 0);
#pragma unroll
    for (int i = 0; i < 2; i++) gx[4 + i] = __builtin_amdgcn_raw_buffer_load_b128(rGW, i * 1024 + l * 16, 0, 0);
    SB();
    asm volatile("s_barrier" ::: "memory");
    SB();
    {
      const float z = add16(row_sum(add32(dot8(gx[0], gx[4]) + dot8(gx[1], gx[5]), dot8(gx[2], gx[4]) + dot8(gx[3], gx[5]))));
      if ((l & 31) == 0) L.gs[l >> 5] = __builtin_amdgcn_rcpf(1.0f + __builtin_amdgcn_exp2f(-1.4426950408889634f * z));
    }
    float hv = 0.f;
    await<true>(&L.fl[1], 4);
    const unsigned tag = L.tag;
    if (ffn) {
#pragma unroll
      for (int i = 0; i < 4; i++) {
        const int u = i * 64 + l;
        const f2 s = (*(f2*)&L.gu[0][u * 2] + *(f2*)&L.gu[1][u * 2]) + (*(f2*)&L.gu[2][u * 2] + *(f2*)&L.gu[3][u * 2]);
        st_sc(ws, HP_OFF + ((((jb * 4 + ((u >> 5) & 3)) * 8 + xc) * 2 + (i >> 1)) * 32 + (l & 31)) * 16, u4{asu(s.x), tag, asu(s.y), tag});
      }
      STAMP(1);
      u4 hp[8];
      __builtin_amdgcn_s_sleep(SLP0);
      poll<8, 1024>(hp, ws, HP_OFF + rr * 8192 + l * 16, tag);
      STAMP(2);
      float gg = 0.f, uu = 0.f;
#pragma unroll
      for (int i = 0; i < 8; i++) { gg += asf(hp[i].x); uu += asf(hp[i].z); }
      const float z2 = 2.0f * 0.7978845608028654f * (gg + 0.044715f * gg * gg * gg);
      hv = gg * __builtin_amdgcn_rcpf(1.0f + __builtin_amdgcn_exp2f(-1.4426950408889634f * z2)) * uu;
    }
    L.hs[l >> 5][l & 31] = (unsigned short)pk(hv, 0.f);
    sig(&L.fl[4], l);
    STAMP(3);
    DUMP(16);
  }
}

extern "C" void kernel_launch(void* const* d_in, const int* in_sizes, int n_in, void* d_out, int out_size, void* d_ws,
                              size_t ws_size, hipStream_t stream) {
  typedef const unsigned short* cp;
  fused_kernel<<<256, 640, 0, stream>>>((cp)d_in[0], (cp)d_in[1], (cp)d_in[2], (cp)d_in[3], (cp)d_in[4], (cp)d_in[6],
                                        (cp)d_in[7], (cp)d_in[8], (cp)d_in[9], (unsigned short*)d_out, (unsigned*)d_ws);
}
```

```cpp
#include <hip/hip_runtime.h>
#include <hip/hip_bf16.h>

typedef __attribute__((ext_vector_type(4))) unsigned u4;
typedef __attribute__((ext_vector_type(2))) unsigned u2;
typedef __attribute__((ext_vector_type(4))) float f4;
typedef __attribute__((ext_vector_type(2))) float f2;
typedef __attribute__((ext_vector_type(8))) __bf16 bf8;
typedef __attribute__((ext_vector_type(2))) __bf16 bf2;
typedef __amdgpu_buffer_rsrc_t rsrc_t;

#define AUX_NT 2
#define AUX_SC1 16
#define SB() __builtin_amdgcn_sched_barrier(0)
#define BAR() asm volatile("s_waitcnt lgkmcnt(0)\n\ts_barrier" ::: "memory")
#define LGKM0() asm volatile("s_waitcnt lgkmcnt(0)" ::: "memory")

constexpr int D = 1024, HQ = 32, HKV = 4, SK = 32768, DH = 128, WIN = 4096, F = 3584;
constexpr int QP_OFF = 4096;
constexpr int QF_OFF = QP_OFF + 32 * 8 * 8 * 16 * 16;
constexpr int OP_OFF = QF_OFF + 256 * 4096;
constexpr int LS_OFF = OP_OFF + 128 * 2 * 32 * 8 * 16;
constexpr int HP_OFF = LS_OFF + 128 * 1024;
constexpr int RP_OFF = HP_OFF + 112 * 8 * 2 * 32 * 16;
constexpr int WS_BYTES = RP_OFF + 256 * 128 * 4 * 16;
constexpr int CODE_BYTES = 12800;
constexpr int SPIN_CAP = 1 << 20;
#define WA 5
#define WF 5
#define SLP 6
#define SLP1 20
#define WX 2
#define SLP0 8

__device__ __forceinline__ rsrc_t mk(const void* p, unsigned bytes) {
  return __builtin_amdgcn_make_buffer_rsrc((void*)p, 0, (int)bytes, 0x00020000);
}
__device__ __forceinline__ u4 ld_nt(rsrc_t r, int off) { return __builtin_amdgcn_raw_buffer_load_b128(r, off, 0, AUX_NT); }
__device__ __forceinline__ u4 ld_sc(rsrc_t r, int off) { return __builtin_amdgcn_raw_buffer_load_b128(r, off, 0, AUX_SC1); }
__device__ __forceinline__ void st_sc(rsrc_t r, int off, u4 v) { __builtin_amdgcn_raw_buffer_store_b128(v, r, off, 0, AUX_SC1); }
__device__ __forceinline__ float asf(unsigned u) { return __builtin_bit_cast(float, u); }
__device__ __forceinline__ unsigned asu(float f) { return __builtin_bit_cast(unsigned, f); }
__device__ __forceinline__ unsigned pk(float a, float b) {
  f2 v = {a, b};
  return __builtin_bit_cast(unsigned, __builtin_convertvector(v, bf2));
}
__device__ __forceinline__ float blo(unsigned u) { return asf(u << 16); }
__device__ __forceinline__ float bhi(unsigned u) { return asf(u & 0xffff0000u); }
__device__ __forceinline__ f4 mfma(u4 a, u4 b, f4 c) {
  return __builtin_amdgcn_mfma_f32_16x16x32_bf16(__builtin_bit_cast(bf8, a), __builtin_bit_cast(bf8, b), c, 0, 0, 0);
}
__device__ __forceinline__ void tile_mma(u4 a, const u4 (&w)[8], f4 (&acc)[8]) {
#pragma unroll
  for (int j = 0; j < 8; j++) {
    const unsigned sel = (j & 1) ? 0x07060302u : 0x05040100u;
    u4 b;
    b.x = __builtin_amdgcn_perm(w[1][j >> 1], w[0][j >> 1], sel);
    b.y = __builtin_amdgcn_perm(w[3][j >> 1], w[2][j >> 1], sel);
    b.z = __builtin_amdgcn_perm(w[5][j >> 1], w[4][j >> 1], sel);
    b.w = __builtin_amdgcn_perm(w[7][j >> 1], w[6][j >> 1], sel);
    acc[j] = mfma(a, b, acc[j]);
  }
}
template <int N>
__device__ __forceinline__ bool tags_ok(const u4 (&v)[N], unsigned tag) {
  unsigned bad = 0;
#pragma unroll
  for (int i = 0; i < N; i++) bad |= (v[i].y ^ tag) | (v[i].w ^ tag);
  return __builtin_amdgcn_ballot_w64(bad != 0) == 0;
}
template <int N, int STRIDE>
__device__ __forceinline__ void poll(u4 (&v)[N], rsrc_t ws, int off, unsigned tag) {
#pragma nounroll
  for (int spin = 0; spin < SPIN_CAP; spin++) {
    asm volatile("" ::: "memory");
#pragma unroll
    for (int i = 0; i < N; i++) v[i] = ld_sc(ws, off + i * STRIDE);
    if (tags_ok(v, tag)) return;
    __builtin_amdgcn_s_sleep(SLP);
  }
}
__device__ __forceinline__ void to_frags(u4 (&w)[8]) {
  u4 b[8];
#pragma unroll
  for (int j = 0; j < 8; j++) {
    const unsigned sel = (j & 1) ? 0x07060302u : 0x05040100u;
#pragma unroll
    for (int k = 0; k < 4; k++) b[j][k] = __builtin_amdgcn_perm(w[2 * k + 1][j >> 1], w[2 * k][j >> 1], sel);
  }
#pragma unroll
  for (int j = 0; j < 8; j++) { w[j] = b[j]; asm volatile("" : "+v"(w[j])); }
}
template <int CTRL>
__device__ __forceinline__ float dpp(float v) {
  return asf(__builtin_amdgcn_update_dpp(0u, asu(v), CTRL, 0xf, 0xf, true));
}
__device__ __forceinline__ float add32(float a, float b) {
  const u2 r = __builtin_amdgcn_permlane32_swap(asu(a), asu(b), false, false);
  return asf(r.x) + asf(r.y);
}
__device__ __forceinline__ float add16(float a) {
  const u2 r = __builtin_amdgcn_permlane16_swap(asu(a), asu(a), false, false);
  return asf(r.x) + asf(r.y);
}
__device__ __forceinline__ float max32(float a) {
  const u2 r = __builtin_amdgcn_permlane32_swap(asu(a), asu(a), false, false);
  return fmaxf(asf(r.x), asf(r.y));
}
__device__ __forceinline__ float max16(float a) {
  const u2 r = __builtin_amdgcn_permlane16_swap(asu(a), asu(a), false, false);
  return fmaxf(asf(r.x), asf(r.y));
}
__device__ __forceinline__ float row_sum(float v) {
  v += dpp<0x128>(v); v += dpp<0x124>(v); v += dpp<0x122>(v); v += dpp<0x121>(v);
  return v;
}
__device__ __forceinline__ float row_max(float v) {
  v = fmaxf(v, dpp<0x128>(v)); v = fmaxf(v, dpp<0x124>(v)); v = fmaxf(v, dpp<0x122>(v)); v = fmaxf(v, dpp<0x121>(v));
  return v;
}
__device__ __forceinline__ f4 sel4(bool p, f4 a, f4 b) {
  return f4{p ? a.x : b.x, p ? a.y : b.y, p ? a.z : b.z, p ? a.w : b.w};
}
__device__ __forceinline__ float dot8(u4 a, u4 b) {
  float s = 0.f;
#pragma unroll
  for (int i = 0; i < 4; i++) s += blo(a[i]) * blo(b[i]) + bhi(a[i]) * bhi(b[i]);
  return s;
}
#define STAMP(k)
#define DUMP(base)

struct Lds {
  u4 kt[7][32 * 16];
  float red[4][8 * 128];
  float qr[4][2 * 128];
  float gu[4][2 * 128 * 2];
  float ml[4][16];
  float gs[2];
  unsigned tag, spare;
  float fin[4][16];
  unsigned fl[16];
  unsigned qs[8 * 68];
  unsigned os[2][16];
  unsigned short hs[2][32];
};
__device__ __forceinline__ void sig(unsigned* f, int l) {
  LGKM0();
  if (l == 0) __hip_atomic_fetch_add(f, 1u, __ATOMIC_RELAXED, __HIP_MEMORY_SCOPE_WORKGROUP);
  asm volatile("" ::: "memory");
}
template <bool SLEEP = false>
__device__ __forceinline__ void await(unsigned* f, unsigned n) {
#pragma nounroll
  for (int spin = 0; spin < SPIN_CAP * 16; spin++) {
    if (__builtin_amdgcn_readfirstlane(__hip_atomic_load(f, __ATOMIC_RELAXED, __HIP_MEMORY_SCOPE_WORKGROUP)) >= n) break;
    if (SLEEP) __builtin_amdgcn_s_sleep(1);
  }
  asm volatile("" ::: "memory");
}

__global__ __launch_bounds__(640, 1) void fused_kernel(
    const unsigned short* __restrict__ x, const unsigned short* __restrict__ kc, const unsigned short* __restrict__ vc,
    const unsigned short* __restrict__ wq, const unsigned short* __restrict__ wo, const unsigned short* __restrict__ wg,
    const unsigned short* __restrict__ wu, const unsigned short* __restrict__ wd, const unsigned short* __restrict__ gw,
    unsigned short* __restrict__ out, unsigned* wsp) {
  __shared__ Lds L;
  STAMP(0);
  const int t = threadIdx.x, w = __builtin_amdgcn_readfirstlane(t >> 6), l = t & 63, c = l & 15, g = l >> 4;
  unsigned pf = 0;
  if (w >= 8) pf = __builtin_amdgcn_raw_buffer_load_b32(mk((const void*)__builtin_amdgcn_s_getpc(), CODE_BYTES), (t - 512) * 128, 0, 0);
  SB();
  const int xc = blockIdx.x & 7, jb = blockIdx.x >> 3;
  const bool ffn = jb < 28;
  const rsrc_t ws = mk(wsp, WS_BYTES);
  const int ab = xc >> 2, akv = xc & 3;
  const int rr = jb * 4 + (xc >> 1);
  const rsrc_t rX = mk(x, 2 * D * 2);
  const f4 zero4 = {0.f, 0.f, 0.f, 0.f};

#define ISSUE(LO, HI, WAIT, LD)                                   \
  _Pragma("unroll") for (int m = (LO); m < (HI); m++) {           \
    const int i = m & 7;                                          \
    SB();                                                         \
    if (WAIT) asm volatile("s_waitcnt vmcnt(%0)" ::"n"((WAIT) - 1)); \
    SB();                                                         \
    T[m >> 3][i] = LD;                                            \
    SB();                                                         \
  }
  const int w4 = w & 3;
  const int krow = xc * 128 + w4 * 32 + g * 8;
  const int toff = (rr * 32 + g * 8) * (D * 2) + (xc & 1) * 1024 + w4 * 256 + c * 16;
  if (w < 4) {
    const size_t kvbase = ((size_t)(ab * HKV + akv) * SK + (SK - WIN) + jb * 128) * DH;
    const rsrc_t rK = mk(kc + kvbase, 128 * DH * 2), rV = mk(vc + kvbase, 128 * DH * 2);
    const rsrc_t rQ = mk(wq, D * HQ * DH * 2);
    const int qoff = krow * (HQ * DH * 2) + jb * 256 + c * 16;
    const int koff = (w * 32 + (g ^ jb)) * 256 + c * 16, voff = (w * 32 + ((g * 4) ^ jb)) * 256 + c * 16;
    u4 T[3][8];
    f4 acc[8];
#define LDA (m < 8 ? ld_nt(rQ, qoff + i * (HQ * DH * 2)) : m < 16 ? ld_nt(rK, koff ^ (i * 1024)) : ld_nt(rV, voff ^ ((i >> 2) * 4096 + (i & 3) * 256)))
    ISSUE(0, 4, 0, LDA)
    const u4 xa = __builtin_amdgcn_raw_buffer_load_b128(rX, c < 2 ? (c * D + krow) * 2 : 0x7ffffff0, 0, 0);
    const unsigned tag = __builtin_amdgcn_raw_buffer_load_b32(ws, 0, 0, AUX_SC1) + 1u;
    ISSUE(4, 8, 0, LDA)
    asm volatile("s_barrier" ::: "memory");
    ISSUE(8, 8 + WA, WA, LDA)
    {
#pragma unroll
      for (int j = 0; j < 8; j++) acc[j] = zero4;
      tile_mma(xa, T[0], acc);
      if (g == 0) {
#pragma unroll
        for (int b = 0; b < 2; b++) {
          *(f4*)&L.qr[w][b * 128 + c * 8] = f4{acc[0][b], acc[1][b], acc[2][b], acc[3][b]};
          *(f4*)&L.qr[w][b * 128 + c * 8 + 4] = f4{acc[4][b], acc[5][b], acc[6][b], acc[7][b]};
        }
      }
      if (t == 0) L.tag = tag;
      sig(&L.fl[0], l);
    }
    STAMP(1);
    ISSUE(8 + WA, 24, WA, LDA)
#pragma unroll
    for (int i = 0; i < 8; i++) L.kt[w][(i * 4 + g) * 16 + (c ^ ((i * 4 + g) & 15))] = T[1][i];
    LGKM0();
#pragma unroll
    for (int i = 0; i < 8; i++) T[1][i] = L.kt[w][((i >> 2) * 16 + c) * 16 + (((i & 3) * 4 + g) ^ c)];
    SB();
    asm volatile("s_waitcnt vmcnt(%0)" ::"n"(WX));
    sig(&L.fl[8 + w], l);
    to_frags(T[2]);
    STAMP(2);
    await(&L.fl[3], 1);
    STAMP(3);
    {
        f4 s0 = zero4, s1 = zero4;
#pragma unroll
        for (int s = 0; s < 4; s++) {
          const u4 qf = *(const u4*)&L.qs[(c & 7) * 68 + s * 16 + g * 4];
          s0 = mfma(T[1][s], qf, s0);
          s1 = mfma(T[1][4 + s], qf, s1);
        }
        const float cs = 0.08838834764831845f * 1.4426950408889634f;
        s0 *= cs; s1 *= cs;
        float m = fmaxf(fmaxf(fmaxf(s0[0], s0[1]), fmaxf(s0[2], s0[3])), fmaxf(fmaxf(s1[0], s1[1]), fmaxf(s1[2], s1[3])));
        m = max32(max16(m));
        float p[8];
#pragma unroll
        for (int r = 0; r < 4; r++) { p[r] = __builtin_amdgcn_exp2f(s0[r] - m); p[4 + r] = __builtin_amdgcn_exp2f(s1[r] - m); }
        float ls = ((p[0] + p[1]) + (p[2] + p[3])) + ((p[4] + p[5]) + (p[6] + p[7]));
        const u4 pa = {pk(p[0], p[1]), pk(p[2], p[3]), pk(p[4], p[5]), pk(p[6], p[7])};
#pragma unroll
        for (int j = 0; j < 8; j++) acc[j] = mfma(pa, T[2][j], zero4);
        ls = add16(ls);
        ls = add32(ls, ls);
        if (l < 8) { L.ml[w][l] = m; L.ml[w][8 + l] = ls; }
        const bool hi = g >= 2;
#pragma unroll
        for (int r = 0; r < 4; r++)
          *(f4*)&L.red[w][((g & 1) * 4 + r) * 128 + c * 8 + (hi ? 4 : 0)] =
              f4{hi ? acc[4][r] : acc[0][r], hi ? acc[5][r] : acc[1][r], hi ? acc[6][r] : acc[2][r], hi ? acc[7][r] : acc[3][r]};
      sig(&L.fl[2], l);
    }
    STAMP(4);
    await(&L.fl[2], 4);
    {
      const int hh = t >> 5;
      const float m0 = L.ml[0][hh], m1 = L.ml[1][hh], m2 = L.ml[2][hh], m3 = L.ml[3][hh];
      const float M = fmaxf(fmaxf(m0, m1), fmaxf(m2, m3));
      const float f0 = __builtin_amdgcn_exp2f(m0 - M), f1 = __builtin_amdgcn_exp2f(m1 - M), f2_ = __builtin_amdgcn_exp2f(m2 - M),
                  f3 = __builtin_amdgcn_exp2f(m3 - M);
      const float Lt = f0 * L.ml[0][8 + hh] + f1 * L.ml[1][8 + hh] + f2_ * L.ml[2][8 + hh] + f3 * L.ml[3][8 + hh];
      f4 o = *(f4*)&L.red[0][t * 4] * f0 + *(f4*)&L.red[1][t * 4] * f1 + *(f4*)&L.red[2][t * 4] * f2_ + *(f4*)&L.red[3][t * 4] * f3;
      o *= __builtin_amdgcn_rcpf(Lt);
      st_sc(ws, OP_OFF + (((((akv * 8 + hh) * 4 + ((l >> 3) & 3)) * 2 + ab) * 32 + jb) * 8 + (l & 7)) * 16,
            u4{pk(o[0], o[1]), tag, pk(o[2], o[3]), tag});
      if ((l & 7) == 0)
        st_sc(ws, LS_OFF + ((((akv * 8 + hh) * 4 + ((l >> 3) & 3)) * 2 + ab) * 32 + jb) * 16, u4{asu(M), tag, asu(Lt), tag});
    }
    STAMP(5);
    if (!(blockIdx.x & 1)) {
      const int red = blockIdx.x >> 1;
      await<true>(&L.fl[6], 4);
      u4 rp[4];
      poll<4, 1024>(rp, ws, RP_OFF + red * 16384 + w * 4096 + l * 16, tag);
      STAMP(6);
      float y = add16(add32((asf(rp[0].x) + asf(rp[1].x)) + (asf(rp[2].x) + asf(rp[3].x)),
                            (asf(rp[0].z) + asf(rp[1].z)) + (asf(rp[2].z) + asf(rp[3].z))));
      y += dpp<0x128>(y);
      if ((l & 24) == 0) L.fin[w][(l >> 5) * 8 + (l & 7)] = y;
      sig(&L.fl[7], l);
      if (w == 0) {
        await(&L.fl[7], 4);
        const int col = (red >> 4) * 128 + ((red & 1) * 8 + (l & 7)) * 8 + ((red >> 1) & 7);
        if (l < 16) out[(l >> 3) * D + col] = (unsigned short)pk((L.fin[0][l] + L.fin[1][l]) + (L.fin[2][l] + L.fin[3][l]), 0.f);
        if (blockIdx.x == 0 && l == 0) __builtin_amdgcn_raw_buffer_store_b32(tag, ws, 0, 0, AUX_SC1);
      }
      STAMP(7);
    }
    if (w == 0) DUMP(0);
  } else if (w < 8) {
    const rsrc_t rG = mk(wg, ffn ? D * F * 2 : 0), rU = mk(wu, ffn ? D * F * 2 : 0), rD = mk(wd, ffn ? F * D * 2 : 0);
    const rsrc_t rO = mk(wo, HQ * DH * D * 2);
    const int goff = krow * (F * 2) + jb * 256 + c * 16;
    u4 T[4][8];
    f4 ag[8], acc[8];
#define LDF (m < 8 ? ld_nt(rG, goff + i * (F * 2)) : m < 16 ? ld_nt(rU, goff + i * (F * 2)) : m < 24 ? ld_nt(rO, toff + i * (D * 2)) : ld_nt(rD, toff + i * (D * 2)))
    const u4 xa = __builtin_amdgcn_raw_buffer_load_b128(rX, c < 2 ? (c * D + krow) * 2 : 0x7ffffff0, 0, 0);
    asm volatile("s_barrier" ::: "memory");
    await<true>(&L.fl[8 + w4], 1);
    ISSUE(0, WF, 0, LDF)
    ISSUE(WF, 8 + WF, WF, LDF)
#pragma unroll
    for (int j = 0; j < 8; j++) ag[j] = zero4;
    tile_mma(xa, T[0], ag);
    ISSUE(8 + WF, 16 + WF, WF, LDF)
#pragma unroll
    for (int j = 0; j < 8; j++) acc[j] = zero4;
    tile_mma(xa, T[1], acc);
    if (g == 0) {
#pragma unroll
      for (int b = 0; b < 2; b++)
#pragma unroll
        for (int j = 0; j < 8; j += 2)
          *(f4*)&L.gu[w4][(b * 128 + c * 8 + j) * 2] = f4{ag[j][b], acc[j][b], ag[j + 1][b], acc[j + 1][b]};
    }
    sig(&L.fl[1], l);
    ISSUE(16 + WF, 32, WF, LDF)
    to_frags(T[2]);
    to_frags(T[3]);
    STAMP(6);
    await(&L.fl[4], 1);
    const unsigned tag = L.tag;
    const float g0 = L.gs[0], g1 = L.gs[1];
    u4 ha = *(const u4*)&L.hs[c & 1][g * 8];
    if (!(c & 2)) ha = u4{0u, 0u, 0u, 0u};
#pragma unroll
    for (int j = 0; j < 8; j++) acc[j] = mfma(ha, T[3][j], zero4);
    await(&L.fl[5], 1);
    u4 oa = *(const u4*)&L.os[c & 1][g * 4];
    if (c & 2) oa = u4{0u, 0u, 0u, 0u};
#pragma unroll
    for (int j = 0; j < 8; j++) acc[j] = mfma(oa, T[2][j], acc[j]);
#pragma unroll
    for (int e = 0; e < 2; e++) {
      const f4 v = sel4(g & 2, sel4(g & 1, acc[e * 4 + 3], acc[e * 4 + 2]), sel4(g & 1, acc[e * 4 + 1], acc[e * 4]));
      const float y0 = g0 * v[0] + (1.0f - g0) * v[2], y1 = g1 * v[1] + (1.0f - g1) * v[3];
      const int red = (((xc & 1) * 4 + w4) * 8 + e * 4 + g) * 2 + (c >> 3);
      st_sc(ws, RP_OFF + ((red * 128 + rr) * 8 + (c & 7)) * 16, u4{asu(y0), tag, asu(y1), tag});
    }
    sig(&L.fl[6], l);
    STAMP(7);
  } else if (w == 8) {
    if (l < 16) L.fl[l] = 0u;
    BAR();
    await(&L.fl[0], 4);
    asm volatile("" ::"v"(pf));
    const unsigned tag = L.tag;
#pragma unroll
    for (int i = 0; i < 2; i++) {
      const int dblk = i * 4 + g, o = dblk * 32 + c * 2;
      const f2 s = (*(f2*)&L.qr[0][o] + *(f2*)&L.qr[1][o]) + (*(f2*)&L.qr[2][o] + *(f2*)&L.qr[3][o]);
      st_sc(ws, QP_OFF + (((jb * 8 + dblk) * 8 + xc) * 16 + c) * 16, u4{asu(s.x), tag, asu(s.y), tag});
    }
    STAMP(1);
    {
      u4 qp[2];
      __builtin_amdgcn_s_sleep(SLP0);
      poll<2, 1024>(qp, ws, QP_OFF + (jb * 8 + xc) * 2048 + l * 16, tag);
      const float a = add16(add32(asf(qp[0].x) + asf(qp[1].x), asf(qp[0].z) + asf(qp[1].z)));
      const u2 r = __builtin_amdgcn_permlane32_swap(asu(a), asu(a), false, false);
      const int fo = QF_OFF + (g * 8 + ab * 4 + (jb >> 3)) * 4096 + ((jb & 7) * 64 + akv * 16 + c) * 8;
#pragma unroll
      for (int i = 0; i < 8; i++)
        __builtin_amdgcn_raw_buffer_store_b64(u2{pk(asf(r.x), asf(r.y)), tag}, ws, fo + i * (32 * 4096), 0, AUX_SC1);
    }
    STAMP(2);
    {
      u4 qf[4];
      __builtin_amdgcn_s_sleep(SLP0);
      poll<4, 1024>(qf, ws, QF_OFF + blockIdx.x * 4096 + l * 16, tag);
#pragma unroll
      for (int i = 0; i < 4; i++) *(u2*)&L.qs[(i * 2 + (l >> 5)) * 68 + (l & 31) * 2] = u2{qf[i].x, qf[i].z};
      sig(&L.fl[3], l);
    }
    STAMP(3);
    await(&L.fl[2], 4);
    STAMP(4);
    {
      u4 lp[1], op[8];
      __builtin_amdgcn_s_sleep(SLP1);
      poll<1, 0>(lp, ws, LS_OFF + rr * 1024 + l * 16, tag);
      STAMP(5);
      const int ooff = OP_OFF + rr * 8192 + l * 16;
#pragma unroll
      for (int i = 0; i < 8; i++) op[i] = ld_sc(ws, ooff + i * 1024);
      SB();
      const float ms = asf(lp[0].x), M = max16(row_max(ms));
      float wt = asf(lp[0].z) * __builtin_amdgcn_exp2f(ms - M);
      wt *= __builtin_amdgcn_rcpf(add16(row_sum(wt)));
      float wi[8];
#pragma unroll
      for (int i = 0; i < 8; i++) wi[i] = asf(__builtin_amdgcn_ds_bpermute((i * 8 + (l >> 3)) * 4, asu(wt)));
      SB();
      if (!tags_ok(op, tag)) poll<8, 1024>(op, ws, ooff, tag);
      STAMP(6);
      f4 o[2] = {zero4, zero4};
#pragma unroll
      for (int i = 0; i < 8; i++) o[i >> 2] += f4{blo(op[i].x), bhi(op[i].x), blo(op[i].z), bhi(op[i].z)} * wi[i];
      float v[4];
#pragma unroll
      for (int k = 0; k < 4; k++) { v[k] = add16(add32(o[0][k], o[1][k])); v[k] += dpp<0x128>(v[k]); }
      if ((l & 24) == 0) *(u2*)&L.os[l >> 5][(l & 7) * 2] = u2{pk(v[0], v[1]), pk(v[2], v[3])};
      sig(&L.fl[5], l);
    }
    STAMP(7);
    DUMP(8);
  } else {
    const rsrc_t rGW = mk(gw, D * 2);
    u4 gx[6];
#pragma unroll
    for (int i = 0; i < 4; i++) gx[i] = __builtin_amdgcn_raw_buffer_load_b128(rX, i * 1024 + l * 16, 0, 0);
#pragma unroll
    for (int i = 0; i < 2; i++) gx[4 + i] = __builtin_amdgcn_raw_buffer_load_b128(rGW, i * 1024 + l * 16, 0, 0);
    SB();
    asm volatile("s_barrier" ::: "memory");
    SB();
    {
      const float z = add16(row_sum(add32(dot8(gx[0], gx[4]) + dot8(gx[1], gx[5]), dot8(gx[2], gx[4]) + dot8(gx[3], gx[5]))));
      if ((l & 31) == 0) L.gs[l >> 5] = __builtin_amdgcn_rcpf(1.0f + __builtin_amdgcn_exp2f(-1.4426950408889634f * z));
    }
    asm volatile("" ::"v"(pf));
    float hv = 0.f;
    await<true>(&L.fl[1], 4);
    const unsigned tag = L.tag;
    if (ffn) {
#pragma unroll
      for (int i = 0; i < 4; i++) {
        const int u = i * 64 + l;
        const f2 s = (*(f2*)&L.gu[0][u * 2] + *(f2*)&L.gu[1][u * 2]) + (*(f2*)&L.gu[2][u * 2] + *(f2*)&L.gu[3][u * 2]);
        st_sc(ws, HP_OFF + ((((jb * 4 + ((u >> 5) & 3)) * 8 + xc) * 2 + (i >> 1)) * 32 + (l & 31)) * 16, u4{asu(s.x), tag, asu(s.y), tag});
      }
      STAMP(1);
      u4 hp[8];
      __builtin_amdgcn_s_sleep(SLP0);
      poll<8, 1024>(hp, ws, HP_OFF + rr * 8192 + l * 16, tag);
      STAMP(2);
      float gg = 0.f, uu = 0.f;
#pragma unroll
      for (int i = 0; i < 8; i++) { gg += asf(hp[i].x); uu += asf(hp[i].z); }
      const float z2 = 2.0f * 0.7978845608028654f * (gg + 0.044715f * gg * gg * gg);
      hv = gg * __builtin_amdgcn_rcpf(1.0f + __builtin_amdgcn_exp2f(-1.4426950408889634f * z2)) * uu;
    }
    L.hs[l >> 5][l & 31] = (unsigned short)pk(hv, 0.f);
    sig(&L.fl[4], l);
    STAMP(3);
    DUMP(16);
  }
}

extern "C" void kernel_launch(void* const* d_in, const int* in_sizes, int n_in, void* d_out, int out_size, void* d_ws,
                              size_t ws_size, hipStream_t stream) {
  typedef const unsigned short* cp;
  fused_kernel<<<256, 640, 0, stream>>>((cp)d_in[0], (cp)d_in[1], (cp)d_in[2], (cp)d_in[3], (cp)d_in[4], (cp)d_in[6],
                                        (cp)d_in[7], (cp)d_in[8], (cp)d_in[9], (unsigned short*)d_out, (unsigned*)d_ws);
}
```

```cpp
#include <hip/hip_runtime.h>
#include <hip/hip_bf16.h>

typedef __attribute__((ext_vector_type(4))) unsigned u4;
typedef __attribute__((ext_vector_type(2))) unsigned u2;
typedef __attribute__((ext_vector_type(4))) float f4;
typedef __attribute__((ext_vector_type(2))) float f2;
typedef __attribute__((ext_vector_type(8))) __bf16 bf8;
typedef __attribute__((ext_vector_type(2))) __bf16 bf2;
typedef __amdgpu_buffer_rsrc_t rsrc_t;

#define AUX_NT 2
#define AUX_SC1 16
#define SB() __builtin_amdgcn_sched_barrier(0)
#define BAR() asm volatile("s_waitcnt lgkmcnt(0)\n\ts_barrier" ::: "memory")
#define LGKM0() asm volatile("s_waitcnt lgkmcnt(0)" ::: "memory")

constexpr int D = 1024, HQ = 32, HKV = 4, SK = 32768, DH = 128, WIN = 4096, F = 3584;
constexpr int QP_OFF = 4096;
constexpr int QF_OFF = QP_OFF + 32 * 8 * 8 * 16 * 16;
constexpr int OP_OFF = QF_OFF + 256 * 4096;
constexpr int HP_OFF = OP_OFF + 128 * 9216;
constexpr int RP_OFF = HP_OFF + 112 * 8 * 2 * 32 * 16;
constexpr int WS_BYTES = RP_OFF + 256 * 128 * 4 * 16;
constexpr int SPIN_CAP = 1 << 20;
constexpr int WA = 5, WF = 5, NC = 4, WF2 = 6, WF1 = 6, WX = 3, SA = 32, SQ = 40, SH = 40, SO = 56, SLP = 4, SLF = 1, SF = 28;

__device__ __forceinline__ rsrc_t mk(const void* p, unsigned bytes) {
  return __builtin_amdgcn_make_buffer_rsrc((void*)p, 0, (int)bytes, 0x00020000);
}
__device__ __forceinline__ u4 ld_nt(rsrc_t r, int off) { return __builtin_amdgcn_raw_buffer_load_b128(r, off, 0, AUX_NT); }
__device__ __forceinline__ u4 ld_sc(rsrc_t r, int off) { return __builtin_amdgcn_raw_buffer_load_b128(r, off, 0, AUX_SC1); }
__device__ __forceinline__ void st_sc(rsrc_t r, int off, u4 v) { __builtin_amdgcn_raw_buffer_store_b128(v, r, off, 0, AUX_SC1); }
__device__ __forceinline__ float asf(unsigned u) { return __builtin_bit_cast(float, u); }
__device__ __forceinline__ unsigned asu(float f) { return __builtin_bit_cast(unsigned, f); }
__device__ __forceinline__ unsigned pk(float a, float b) {
  f2 v = {a, b};
  return __builtin_bit_cast(unsigned, __builtin_convertvector(v, bf2));
}
__device__ __forceinline__ float blo(unsigned u) { return asf(u << 16); }
__device__ __forceinline__ float bhi(unsigned u) { return asf(u & 0xffff0000u); }
__device__ __forceinline__ f4 mfma(u4 a, u4 b, f4 c) {
  return __builtin_amdgcn_mfma_f32_16x16x32_bf16(__builtin_bit_cast(bf8, a), __builtin_bit_cast(bf8, b), c, 0, 0, 0);
}
__device__ __forceinline__ void tile_mma(u4 a, const u4 (&w)[8], f4 (&acc)[8]) {
#pragma unroll
  for (int j = 0; j < 8; j++) {
    const unsigned sel = (j & 1) ? 0x07060302u : 0x05040100u;
    u4 b;
    b.x = __builtin_amdgcn_perm(w[1][j >> 1], w[0][j >> 1], sel);
    b.y = __builtin_amdgcn_perm(w[3][j >> 1], w[2][j >> 1], sel);
    b.z = __builtin_amdgcn_perm(w[5][j >> 1], w[4][j >> 1], sel);
    b.w = __builtin_amdgcn_perm(w[7][j >> 1], w[6][j >> 1], sel);
    acc[j] = mfma(a, b, acc[j]);
  }
}
template <int N>
__device__ __forceinline__ bool tags_ok(const u4 (&v)[N], unsigned tag) {
  unsigned bad = 0;
#pragma unroll
  for (int i = 0; i < N; i++) bad |= (v[i].y ^ tag) | (v[i].w ^ tag);
  return __builtin_amdgcn_ballot_w64(bad != 0) == 0;
}
template <int N, int STRIDE, int FIRST, int RETRY>
__device__ __forceinline__ void poll(u4 (&v)[N], rsrc_t ws, int off, unsigned tag) {
  __builtin_amdgcn_s_sleep(FIRST);
#pragma nounroll
  for (int spin = 0; spin < SPIN_CAP; spin++) {
    asm volatile("" ::: "memory");
#pragma unroll
    for (int i = 0; i < N; i++) v[i] = ld_sc(ws, off + i * STRIDE);
    if (tags_ok(v, tag)) return;
    __builtin_amdgcn_s_sleep(RETRY);
  }
}
__device__ __forceinline__ void to_frags(u4 (&w)[8]) {
  u4 b[8];
#pragma unroll
  for (int j = 0; j < 8; j++) {
    const unsigned sel = (j & 1) ? 0x07060302u : 0x05040100u;
#pragma unroll
    for (int k = 0; k < 4; k++) b[j][k] = __builtin_amdgcn_perm(w[2 * k + 1][j >> 1], w[2 * k][j >> 1], sel);
  }
#pragma unroll
  for (int j = 0; j < 8; j++) { w[j] = b[j]; asm volatile("" : "+v"(w[j])); }
}
template <int CTRL>
__device__ __forceinline__ float dpp(float v) {
  return asf(__builtin_amdgcn_update_dpp(0u, asu(v), CTRL, 0xf, 0xf, true));
}
__device__ __forceinline__ float add32(float a, float b) {
  const u2 r = __builtin_amdgcn_permlane32_swap(asu(a), asu(b), false, false);
  return asf(r.x) + asf(r.y);
}
__device__ __forceinline__ float add16(float a) {
  const u2 r = __builtin_amdgcn_permlane16_swap(asu(a), asu(a), false, false);
  return asf(r.x) + asf(r.y);
}
__device__ __forceinline__ float max32(float a) {
  const u2 r = __builtin_amdgcn_permlane32_swap(asu(a), asu(a), false, false);
  return fmaxf(asf(r.x), asf(r.y));
}
__device__ __forceinline__ float max16(float a) {
  const u2 r = __builtin_amdgcn_permlane16_swap(asu(a), asu(a), false, false);
  return fmaxf(asf(r.x), asf(r.y));
}
__device__ __forceinline__ float row_sum(float v) {
  v += dpp<0x128>(v); v += dpp<0x124>(v); v += dpp<0x122>(v); v += dpp<0x121>(v);
  return v;
}
__device__ __forceinline__ float row_max(float v) {
  v = fmaxf(v, dpp<0x128>(v)); v = fmaxf(v, dpp<0x124>(v)); v = fmaxf(v, dpp<0x122>(v)); v = fmaxf(v, dpp<0x121>(v));
  return v;
}
__device__ __forceinline__ f4 sel4(bool p, f4 a, f4 b) {
  return f4{p ? a.x : b.x, p ? a.y : b.y, p ? a.z : b.z, p ? a.w : b.w};
}
__device__ __forceinline__ float dot8(u4 a, u4 b) {
  float s = 0.f;
#pragma unroll
  for (int i = 0; i < 4; i++) s += blo(a[i]) * blo(b[i]) + bhi(a[i]) * bhi(b[i]);
  return s;
}

struct Lds {
  u4 kt[7][32 * 16];
  float red[4][8 * 128];
  float qr[4][2 * 128];
  float gu[4][2 * 128 * 2];
  float ml[4][16];
  float gs[2];
  unsigned tag, spare;
  float fin[4][16];
  unsigned fl[16];
  unsigned qs[8 * 68];
  unsigned os[2][16];
  unsigned short hs[2][32];
};
__device__ __forceinline__ void sig(unsigned* f, int l) {
  LGKM0();
  if (l == 0) __hip_atomic_fetch_add(f, 1u, __ATOMIC_RELAXED, __HIP_MEMORY_SCOPE_WORKGROUP);
  asm volatile("" ::: "memory");
}
template <bool SLEEP = false>
__device__ __forceinline__ void await(unsigned* f, unsigned n) {
#pragma nounroll
  for (int spin = 0; spin < SPIN_CAP * 16; spin++) {
    if (__builtin_amdgcn_readfirstlane(__hip_atomic_load(f, __ATOMIC_RELAXED, __HIP_MEMORY_SCOPE_WORKGROUP)) >= n) break;
    if (SLEEP) __builtin_amdgcn_s_sleep(1);
  }
  asm volatile("" ::: "memory");
}

__global__ __launch_bounds__(640, 1) void fused_kernel(
    const unsigned short* __restrict__ x, const unsigned short* __restrict__ kc, const unsigned short* __restrict__ vc,
    const unsigned short* __restrict__ wq, const unsigned short* __restrict__ wo, const unsigned short* __restrict__ wg,
    const unsigned short* __restrict__ wu, const unsigned short* __restrict__ wd, const unsigned short* __restrict__ gw,
    unsigned short* __restrict__ out, unsigned* wsp) {
  __shared__ Lds L;
  const int t = threadIdx.x, w = __builtin_amdgcn_readfirstlane(t >> 6), l = t & 63, c = l & 15, g = l >> 4;
  const int xc = blockIdx.x & 7, jb = blockIdx.x >> 3;
  const bool ffn = jb < 28;
  const rsrc_t ws = mk(wsp, WS_BYTES);
  const int ab = xc >> 2, akv = xc & 3;
  const int rr = jb * 4 + (xc >> 1);
  const rsrc_t rX = mk(x, 2 * D * 2);
  const f4 zero4 = {0.f, 0.f, 0.f, 0.f};

#define ISSUE(LO, HI, WAIT, LD)                                   \
  _Pragma("unroll") for (int m = (LO); m < (HI); m++) {           \
    const int i = m & 7;                                          \
    SB();                                                         \
    if (WAIT) asm volatile("s_waitcnt vmcnt(%0)" ::"n"((WAIT) - 1)); \
    SB();                                                         \
    T[m >> 3][i] = LD;                                            \
    SB();                                                         \
  }
  const int w4 = w & 3;
  const int krow = xc * 128 + w4 * 32 + g * 8;
  const int toff = (rr * 32 + g * 8) * (D * 2) + (xc & 1) * 1024 + w4 * 256 + c * 16;
  if (w < 4) {
    const size_t kvbase = ((size_t)(ab * HKV + akv) * SK + (SK - WIN) + jb * 128) * DH;
    const rsrc_t rK = mk(kc + kvbase, 128 * DH * 2), rV = mk(vc + kvbase, 128 * DH * 2);
    const rsrc_t rQ = mk(wq, D * HQ * DH * 2);
    const int qoff = krow * (HQ * DH * 2) + jb * 256 + c * 16;
    const int koff = (w * 32 + (g ^ jb)) * 256 + c * 16, voff = (w * 32 + ((g * 4) ^ jb)) * 256 + c * 16;
    u4 T[3][8];
    f4 acc[8];
#define LDA (m < 8 ? ld_nt(rQ, qoff + i * (HQ * DH * 2)) : m < 16 ? ld_nt(rK, koff ^ (i * 1024)) : ld_nt(rV, voff ^ ((i >> 2) * 4096 + (i & 3) * 256)))
    ISSUE(0, 4, 0, LDA)
    const u4 xa = __builtin_amdgcn_raw_buffer_load_b128(rX, c < 2 ? (c * D + krow) * 2 : 0x7ffffff0, 0, 0);
    const unsigned tag = __builtin_amdgcn_raw_buffer_load_b32(ws, 0, 0, AUX_SC1) + 1u;
    ISSUE(4, 8, 0, LDA)
    asm volatile("s_barrier" ::: "memory");
    ISSUE(8, 8 + WA, WA, LDA)
    {
#pragma unroll
      for (int j = 0; j < 8; j++) acc[j] = zero4;
      tile_mma(xa, T[0], acc);
      if (g == 0) {
#pragma unroll
        for (int b = 0; b < 2; b++) {
          *(f4*)&L.qr[w][b * 128 + c * 8] = f4{acc[0][b], acc[1][b], acc[2][b], acc[3][b]};
          *(f4*)&L.qr[w][b * 128 + c * 8 + 4] = f4{acc[4][b], acc[5][b], acc[6][b], acc[7][b]};
        }
      }
      if (t == 0) L.tag = tag;
      sig(&L.fl[0], l);
    }
    ISSUE(8 + WA, 24, WA, LDA)
#pragma unroll
    for (int i = 0; i < 8; i++) L.kt[w][(i * 4 + g) * 16 + (c ^ ((i * 4 + g) & 15))] = T[1][i];
    LGKM0();
#pragma unroll
    for (int i = 0; i < 8; i++) T[1][i] = L.kt[w][((i >> 2) * 16 + c) * 16 + (((i & 3) * 4 + g) ^ c)];
    SB();
    asm volatile("s_waitcnt vmcnt(%0)" ::"n"(WX));
    sig(&L.fl[8 + w], l);
    to_frags(T[2]);
    await(&L.fl[3], 1);
    {
        f4 s0 = zero4, s1 = zero4;
#pragma unroll
        for (int s = 0; s < 4; s++) {
          const u4 qf = *(const u4*)&L.qs[(c & 7) * 68 + s * 16 + g * 4];
          s0 = mfma(T[1][s], qf, s0);
          s1 = mfma(T[1][4 + s], qf, s1);
        }
        const float cs = 0.08838834764831845f * 1.4426950408889634f;
        s0 *= cs; s1 *= cs;
        float m = fmaxf(fmaxf(fmaxf(s0[0], s0[1]), fmaxf(s0[2], s0[3])), fmaxf(fmaxf(s1[0], s1[1]), fmaxf(s1[2], s1[3])));
        m = max32(max16(m));
        float p[8];
#pragma unroll
        for (int r = 0; r < 4; r++) { p[r] = __builtin_amdgcn_exp2f(s0[r] - m); p[4 + r] = __builtin_amdgcn_exp2f(s1[r] - m); }
        float ls = ((p[0] + p[1]) + (p[2] + p[3])) + ((p[4] + p[5]) + (p[6] + p[7]));
        const u4 pa = {pk(p[0], p[1]), pk(p[2], p[3]), pk(p[4], p[5]), pk(p[6], p[7])};
#pragma unroll
        for (int j = 0; j < 8; j++) acc[j] = mfma(pa, T[2][j], zero4);
        ls = add16(ls);
        ls = add32(ls, ls);
        if (l < 8) { L.ml[w][l] = m; L.ml[w][8 + l] = ls; }
        const bool hi = g >= 2;
#pragma unroll
        for (int r = 0; r < 4; r++)
          *(f4*)&L.red[w][((g & 1) * 4 + r) * 128 + c * 8 + (hi ? 4 : 0)] =
              f4{hi ? acc[4][r] : acc[0][r], hi ? acc[5][r] : acc[1][r], hi ? acc[6][r] : acc[2][r], hi ? acc[7][r] : acc[3][r]};
      sig(&L.fl[2], l);
    }
    await(&L.fl[2], 4);
    {
      const int hh = t >> 5;
      const float m0 = L.ml[0][hh], m1 = L.ml[1][hh], m2 = L.ml[2][hh], m3 = L.ml[3][hh];
      const float M = fmaxf(fmaxf(m0, m1), fmaxf(m2, m3));
      const float f0 = __builtin_amdgcn_exp2f(m0 - M), f1 = __builtin_amdgcn_exp2f(m1 - M), f2_ = __builtin_amdgcn_exp2f(m2 - M),
                  f3 = __builtin_amdgcn_exp2f(m3 - M);
      const float Lt = f0 * L.ml[0][8 + hh] + f1 * L.ml[1][8 + hh] + f2_ * L.ml[2][8 + hh] + f3 * L.ml[3][8 + hh];
      f4 o = *(f4*)&L.red[0][t * 4] * f0 + *(f4*)&L.red[1][t * 4] * f1 + *(f4*)&L.red[2][t * 4] * f2_ + *(f4*)&L.red[3][t * 4] * f3;
      o *= __builtin_amdgcn_rcpf(Lt);
      const int oo = OP_OFF + ((akv * 8 + hh) * 4 + ((l >> 3) & 3)) * 9216;
      st_sc(ws, oo + ((ab * 32 + jb) * 8 + (l & 7)) * 16, u4{pk(o[0], o[1]), tag, pk(o[2], o[3]), tag});
      if ((l & 7) == 0) st_sc(ws, oo + 8192 + (ab * 32 + jb) * 16, u4{asu(M), tag, asu(Lt), tag});
    }
    if (!(blockIdx.x & 1)) {
      const int red = blockIdx.x >> 1;
      await<true>(&L.fl[6], 4);
      u4 rp[4];
      poll<4, 1024, SF, SLF>(rp, ws, RP_OFF + red * 16384 + w * 4096 + l * 16, tag);
      float y = add16(add32((asf(rp[0].x) + asf(rp[1].x)) + (asf(rp[2].x) + asf(rp[3].x)),
                            (asf(rp[0].z) + asf(rp[1].z)) + (asf(rp[2].z) + asf(rp[3].z))));
      y += dpp<0x128>(y);
      if ((l & 24) == 0) L.fin[w][(l >> 5) * 8 + (l & 7)] = y;
      sig(&L.fl[7], l);
      if (w == 0) {
        await(&L.fl[7], 4);
        const int col = (red >> 4) * 128 + ((red & 1) * 8 + (l & 7)) * 8 + ((red >> 1) & 7);
        if (l < 16) out[(l >> 3) * D + col] = (unsigned short)pk((L.fin[0][l] + L.fin[1][l]) + (L.fin[2][l] + L.fin[3][l]), 0.f);
        if (blockIdx.x == 0 && l == 0) __builtin_amdgcn_raw_buffer_store_b32(tag, ws, 0, 0, AUX_SC1);
      }
    }
  } else if (w < 8) {
    const rsrc_t rG = mk(wg, ffn ? D * F * 2 : 0), rU = mk(wu, ffn ? D * F * 2 : 0), rD = mk(wd, ffn ? F * D * 2 : 0);
    const rsrc_t rO = mk(wo, HQ * DH * D * 2);
    const int goff = krow * (F * 2) + jb * 256 + c * 16;
    u4 T[4][8];
    f4 ag[8], acc[8];
#define LDF (m < 8 ? ld_nt(rG, goff + i * (F * 2)) : m < 16 ? ld_nt(rU, goff + i * (F * 2)) : m < 24 ? ld_nt(rO, toff + i * (D * 2)) : ld_nt(rD, toff + i * (D * 2)))
    const u4 xa = __builtin_amdgcn_raw_buffer_load_b128(rX, c < 2 ? (c * D + krow) * 2 : 0x7ffffff0, 0, 0);
    asm volatile("s_barrier" ::: "memory");
    await<true>(&L.fl[8 + w4], 1);
    ISSUE(0, WF, 0, LDF)
    ISSUE(WF, 8 + WF, WF, LDF)
#pragma unroll
    for (int j = 0; j < 8; j++) ag[j] = zero4;
    tile_mma(xa, T[0], ag);
    ISSUE(8 + WF, 16 + WF1, WF1, LDF)
#pragma unroll
    for (int j = 0; j < 8; j++) acc[j] = zero4;
    tile_mma(xa, T[1], acc);
    if (g == 0) {
#pragma unroll
      for (int b = 0; b < 2; b++)
#pragma unroll
        for (int j = 0; j < 8; j += 2)
          *(f4*)&L.gu[w4][(b * 128 + c * 8 + j) * 2] = f4{ag[j][b], acc[j][b], ag[j + 1][b], acc[j + 1][b]};
    }
    sig(&L.fl[1], l);
    ISSUE(16 + WF1, 32, WF2, LDF)
    to_frags(T[2]);
    to_frags(T[3]);
    await(&L.fl[4], 1);
    const unsigned tag = L.tag;
    const float g0 = L.gs[0], g1 = L.gs[1];
    u4 ha = *(const u4*)&L.hs[c & 1][g * 8];
    if (!(c & 2)) ha = u4{0u, 0u, 0u, 0u};
#pragma unroll
    for (int j = 0; j < 8; j++) acc[j] = mfma(ha, T[3][j], zero4);
    await(&L.fl[5], 1);
    u4 oa = *(const u4*)&L.os[c & 1][g * 4];
    if (c & 2) oa = u4{0u, 0u, 0u, 0u};
#pragma unroll
    for (int j = 0; j < 8; j++) acc[j] = mfma(oa, T[2][j], acc[j]);
#pragma unroll
    for (int e = 0; e < 2; e++) {
      const f4 v = sel4(g & 2, sel4(g & 1, acc[e * 4 + 3], acc[e * 4 + 2]), sel4(g & 1, acc[e * 4 + 1], acc[e * 4]));
      const float y0 = g0 * v[0] + (1.0f - g0) * v[2], y1 = g1 * v[1] + (1.0f - g1) * v[3];
      const int red = (((xc & 1) * 4 + w4) * 8 + e * 4 + g) * 2 + (c >> 3);
      st_sc(ws, RP_OFF + ((red * 128 + rr) * 8 + (c & 7)) * 16, u4{asu(y0), tag, asu(y1), tag});
    }
    sig(&L.fl[6], l);
  } else if (w == 8) {
    if (l < 16) L.fl[l] = 0u;
    BAR();
    await(&L.fl[0], 4);
    const unsigned tag = L.tag;
#pragma unroll
    for (int i = 0; i < 2; i++) {
      const int dblk = i * 4 + g, o = dblk * 32 + c * 2;
      const f2 s = (*(f2*)&L.qr[0][o] + *(f2*)&L.qr[1][o]) + (*(f2*)&L.qr[2][o] + *(f2*)&L.qr[3][o]);
      st_sc(ws, QP_OFF + (((jb * 8 + dblk) * 8 + xc) * 16 + c) * 16, u4{asu(s.x), tag, asu(s.y), tag});
    }
    {
      u4 qp[2];
      poll<2, 1024, SA, SLP>(qp, ws, QP_OFF + (jb * 8 + xc) * 2048 + l * 16, tag);
      const float a = add16(add32(asf(qp[0].x) + asf(qp[1].x), asf(qp[0].z) + asf(qp[1].z)));
      const u2 r = __builtin_amdgcn_permlane32_swap(asu(a), asu(a), false, false);
      const int fo = QF_OFF + (g * 8 + ab * 4 + (jb >> 3)) * 4096 + ((jb & 7) * 64 + akv * 16 + c) * 8;
#pragma unroll
      for (int i = 0; i < NC / 4; i++)
        __builtin_amdgcn_raw_buffer_store_b64(u2{pk(asf(r.x), asf(r.y)), tag}, ws, fo + i * (32 * 4096), 0, AUX_SC1);
    }
    {
      u4 qf[4];
      poll<4, 1024, SQ, SLP>(qf, ws, QF_OFF + (blockIdx.x & (NC * 8 - 1)) * 4096 + l * 16, tag);
#pragma unroll
      for (int i = 0; i < 4; i++) *(u2*)&L.qs[(i * 2 + (l >> 5)) * 68 + (l & 31) * 2] = u2{qf[i].x, qf[i].z};
      sig(&L.fl[3], l);
    }
    await(&L.fl[2], 4);
    {
      u4 op[9];
      poll<9, 1024, SO, SLP>(op, ws, OP_OFF + rr * 9216 + l * 16, tag);
      const float ms = asf(op[8].x), M = max16(row_max(ms));
      float wt = asf(op[8].z) * __builtin_amdgcn_exp2f(ms - M);
      wt *= __builtin_amdgcn_rcpf(add16(row_sum(wt)));
      float wi[8];
#pragma unroll
      for (int i = 0; i < 8; i++) wi[i] = asf(__builtin_amdgcn_ds_bpermute((i * 8 + (l >> 3)) * 4, asu(wt)));
      f4 o[2] = {zero4, zero4};
#pragma unroll
      for (int i = 0; i < 8; i++) o[i >> 2] += f4{blo(op[i].x), bhi(op[i].x), blo(op[i].z), bhi(op[i].z)} * wi[i];
      float v[4];
#pragma unroll
      for (int k = 0; k < 4; k++) { v[k] = add16(add32(o[0][k], o[1][k])); v[k] += dpp<0x128>(v[k]); }
      if ((l & 24) == 0) *(u2*)&L.os[l >> 5][(l & 7) * 2] = u2{pk(v[0], v[1]), pk(v[2], v[3])};
      sig(&L.fl[5], l);
    }
  } else {
    const rsrc_t rGW = mk(gw, D * 2);
    u4 gx[6];
#pragma unroll
    for (int i = 0; i < 4; i++) gx[i] = __builtin_amdgcn_raw_buffer_load_b128(rX, i * 1024 + l * 16, 0, 0);
#pragma unroll
    for (int i = 0; i < 2; i++) gx[4 + i] = __builtin_amdgcn_raw_buffer_load_b128(rGW, i * 1024 + l * 16, 0, 0);
    SB();
    asm volatile("s_barrier" ::: "memory");
    SB();
    {
      const float z = add16(row_sum(add32(dot8(gx[0], gx[4]) + dot8(gx[1], gx[5]), dot8(gx[2], gx[4]) + dot8(gx[3], gx[5]))));
      if ((l & 31) == 0) L.gs[l >> 5] = __builtin_amdgcn_rcpf(1.0f + __builtin_amdgcn_exp2f(-1.4426950408889634f * z));
    }
    float hv = 0.f;
    await<true>(&L.fl[1], 4);
    const unsigned tag = L.tag;
    if (ffn) {
#pragma unroll
      for (int i = 0; i < 4; i++) {
        const int u = i * 64 + l;
        const f2 s = (*(f2*)&L.gu[0][u * 2] + *(f2*)&L.gu[1][u * 2]) + (*(f2*)&L.gu[2][u * 2] + *(f2*)&L.gu[3][u * 2]);
        st_sc(ws, HP_OFF + ((((jb * 4 + ((u >> 5) & 3)) * 8 + xc) * 2 + (i >> 1)) * 32 + (l & 31)) * 16, u4{asu(s.x), tag, asu(s.y), tag});
      }
      u4 hp[8];
      poll<8, 1024, SH, SLP>(hp, ws, HP_OFF + rr * 8192 + l * 16, tag);
      float gg = 0.f, uu = 0.f;
#pragma unroll
      for (int i = 0; i < 8; i++) { gg += asf(hp[i].x); uu += asf(hp[i].z); }
      const float z2 = 2.0f * 0.7978845608028654f * (gg + 0.044715f * gg * gg * gg);
      hv = gg * __builtin_amdgcn_rcpf(1.0f + __builtin_amdgcn_exp2f(-1.4426950408889634f * z2)) * uu;
    }
    L.hs[l >> 5][l & 31] = (unsigned short)pk(hv, 0.f);
    sig(&L.fl[4], l);
  }
}

extern "C" void kernel_launch(void* const* d_in, const int* in_sizes, int n_in, void* d_out, int out_size, void* d_ws,
                              size_t ws_size, hipStream_t stream) {
  typedef const unsigned short* cp;
  fused_kernel<<<256, 640, 0, stream>>>((cp)d_in[0], (cp)d_in[1], (cp)d_in[2], (cp)d_in[3], (cp)d_in[4], (cp)d_in[6],
                                        (cp)d_in[7], (cp)d_in[8], (cp)d_in[9], (unsigned short*)d_out, (unsigned*)d_ws);
}
```
